```python
import torch

def _softmax(x, axis=-1):
    return torch.softmax(x, dim=axis)
def _gelu(x):
    return torch.nn.functional.gelu(x, approximate="tanh")
def _silu(x):
    return torch.nn.functional.silu(x)
def _sigmoid(x):
    return torch.sigmoid(x)
def _layernorm(x, axis=-1, eps=1e-5):
    m = x.mean(dim=axis, keepdim=True)
    v = x.var(dim=axis, keepdim=True, unbiased=False)
    return (x - m) / torch.sqrt(v + eps)
def _scatter_add(tgt, idx, src, axis=0):
    return tgt.clone().index_add_(axis, idx, src)
def _segment_sum(src, seg, n):
    out = torch.zeros((n,) + src.shape[1:], dtype=src.dtype)
    return out.index_add_(0, seg, src)
def _pad(x, pw):
    flat = tuple(v for pair in reversed(pw) for v in pair)
    return torch.nn.functional.pad(x, flat)

kernel_name = 'parallel_sum__gqa_block_cached__dense_activation_squared_relu__decode'

def setup_inputs():

    _w = (lambda a: (lambda t: t.to(torch.bfloat16) if t.is_floating_point() else t)(torch.as_tensor(a)))
    def _A_setup():
        B, Sq, D = 8, 1, 512
        Hq, Hkv, Sk, Dh = 32, 32, 4096, 128
        assert Hq % Hkv == 0, (Hq, Hkv)
        return {
            "x": _w((lambda *s: torch.randn(*s, dtype=torch.bfloat16) * 0.05)(B, Sq, D)),
            "k_cache": _w((lambda *s: torch.randn(*s, dtype=torch.bfloat16) * 1.6)(B, Hkv, Sk, Dh)),
            "v_cache": _w((lambda *s: torch.randn(*s, dtype=torch.bfloat16) * 3.2)(B, Hkv, Sk, Dh)),
            "wq": _w((lambda *s: torch.randn(*s, dtype=torch.bfloat16) * 1.6)(D, Hq * Dh) * (D ** -0.5)),
            "wo": _w((lambda *s: torch.randn(*s, dtype=torch.bfloat16) * 3.2)(Hq * Dh, D) * ((Hq * Dh) ** -0.5)),
        }
    _A_in = _A_setup()

    def _B_setup():
        B, S, D, F = 8, 1, 512, 1376
        return {
            "x": _w((lambda *s: torch.randn(*s, dtype=torch.bfloat16) * 0.05)(B, S, D)),
            "w1": _w((lambda *s: torch.randn(*s, dtype=torch.bfloat16) * 0.05)(D, F)),
            "b1": _w((lambda *s: torch.randn(*s, dtype=torch.bfloat16) * 0.4)(F)),
            "w2": _w((lambda *s: torch.randn(*s, dtype=torch.bfloat16) * 0.05)(F, D)),
            "b2": _w((lambda *s: torch.randn(*s, dtype=torch.bfloat16) * 1.6)(D)),
        }
    _B_in = _B_setup()

    return {
        "A_x": _A_in["x"],
        "A_k_cache": _A_in["k_cache"],
        "A_v_cache": _A_in["v_cache"],
        "A_wq": _A_in["wq"],
        "A_wo": _A_in["wo"],
        "B_x": _B_in["x"],
        "B_w1": _B_in["w1"],
        "B_b1": _B_in["b1"],
        "B_w2": _B_in["w2"],
        "B_b2": _B_in["b2"],
    }

def reference(A_x, A_k_cache, A_v_cache, A_wq, A_wo, B_x, B_w1, B_b1, B_w2, B_b2):
    def _A_ref(x, k_cache, v_cache, wq, wo):
        B, Sq, D = x.shape
        Hq, Dh = 32, 128
        Hkv = k_cache.shape[1]
        rep = Hq // Hkv
        q = (lambda x, *d: x.permute(*d))((x @ wq).reshape(B, Sq, Hq, Dh), 0, 2, 1, 3)
        k = (lambda x, n, axis: torch.repeat_interleave(x, n, dim=axis))(k_cache, rep, axis=1)
        v = (lambda x, n, axis: torch.repeat_interleave(x, n, dim=axis))(v_cache, rep, axis=1)
        scores = torch.einsum("bhqd,bhkd->bhqk", q, k) * (Dh ** -0.5)
        p = _softmax(scores, axis=-1)
        o = torch.einsum("bhqk,bhkd->bhqd", p, v)
        return (lambda x, *d: x.permute(*d))(o, 0, 2, 1, 3).reshape(B, Sq, Hq * Dh) @ wo

    def _B_ref(x, w1, b1, w2, b2):
        h = torch.einsum("bsd,df->bsf", x, w1) + b1
        zero = torch.zeros_like(h)
        r = torch.where(h > zero, h, zero)
        h = r * r
        out = torch.einsum("bsf,fd->bsd", h, w2) + b2
        return out

    x = A_x
    xn = _layernorm(x, axis=-1)
    return x + _A_ref(xn, A_k_cache, A_v_cache, A_wq, A_wo) + _B_ref(xn, B_w1, B_b1, B_w2, B_b2)
```

```cpp
#include <hip/hip_runtime.h>
#include <hip/hip_bf16.h>
#include <stdint.h>

typedef unsigned u32x4 __attribute__((ext_vector_type(4)));
typedef unsigned u32x2 __attribute__((ext_vector_type(2)));
typedef float f32x4 __attribute__((ext_vector_type(4)));
typedef float f32x2 __attribute__((ext_vector_type(2)));
typedef __bf16 bf16x2 __attribute__((ext_vector_type(2)));
typedef unsigned short u16;
typedef unsigned long long u64;
typedef __amdgpu_buffer_rsrc_t rsrc_t;

constexpr int NB = 8, NH = 32, SK = 4096, DH = 128, DM = 512, FF = 1376;
constexpr unsigned HEAD_BYTES = SK * DH * 2;
constexpr unsigned WQ_ROW = NH * DH * 2;

#define THR 5
#define HTHR 7
#define SCH_A 690
#define SCH_B 214
#define STR2(x) #x
#define STR(x) STR2(x)
#define THROTTLE() asm volatile("s_waitcnt vmcnt(" STR(THR) ")" ::: "memory")
#define THROTTLE_LATE(late) asm volatile("s_waitcnt vmcnt(" STR(HTHR) ")\n\ts_cmp_gt_i32 %0, 0\n\ts_cbranch_scc1 1f\n\ts_waitcnt vmcnt(" STR(THR) ")\n1:" :: "s"(late) : "memory", "scc")
#define LATE(n) ((unsigned)__builtin_amdgcn_s_memrealtime() - tstart - (unsigned)(SCH_A + SCH_B * (n)))
#define DTHR 6
constexpr int PACE = 16, POLL_FIRST = 32, POLL_SLEEP = 1;
constexpr int SC1 = 16, NT = 2;

static __device__ __forceinline__ float dot2(unsigned a, unsigned b, float c) {
  return __builtin_amdgcn_fdot2_f32_bf16(__builtin_bit_cast(bf16x2, a), __builtin_bit_cast(bf16x2, b), c, false);
}
static __device__ __forceinline__ float bflo(unsigned u) { return __builtin_bit_cast(float, u << 16); }
static __device__ __forceinline__ float bfhi(unsigned u) { return __builtin_bit_cast(float, u & 0xffff0000u); }
static __device__ __forceinline__ unsigned cvtpk(float lo, float hi) {
  bf16x2 r = {(__bf16)lo, (__bf16)hi};
  return __builtin_bit_cast(unsigned, r);
}
static __device__ __forceinline__ float rbf(float x) { return (float)(__bf16)x; }
template <int CTRL> static __device__ __forceinline__ float dpp(float x) {
  return __builtin_bit_cast(float, __builtin_amdgcn_mov_dpp(__builtin_bit_cast(int, x), CTRL, 0xf, 0xf, true));
}
template <int N> static __device__ __forceinline__ unsigned rowbc(unsigned x) {
  return (unsigned)__builtin_amdgcn_mov_dpp((int)x, 0x150 + N, 0xf, 0xf, true);
}
static __device__ __forceinline__ float row16_sum(float x) { x += dpp<0x128>(x); x += dpp<0x124>(x); x += dpp<0x122>(x); x += dpp<0x121>(x); return x; }
static __device__ __forceinline__ float row16_max(float x) { x = fmaxf(x, dpp<0x128>(x)); x = fmaxf(x, dpp<0x124>(x)); x = fmaxf(x, dpp<0x122>(x)); x = fmaxf(x, dpp<0x121>(x)); return x; }
static __device__ __forceinline__ float sw32(float a, float b) {
  auto t = __builtin_amdgcn_permlane32_swap(__float_as_uint(a), __float_as_uint(b), false, false);
  return __uint_as_float(t[0]) + __uint_as_float(t[1]);
}
static __device__ __forceinline__ float sw16(float a, float b) {
  auto t = __builtin_amdgcn_permlane16_swap(__float_as_uint(a), __float_as_uint(b), false, false);
  return __uint_as_float(t[0]) + __uint_as_float(t[1]);
}
static __device__ __forceinline__ float xrow_sum(float x) { x = sw16(x, x); return sw32(x, x); }
static __device__ __forceinline__ float xrow_max(float x) {
  auto s = __builtin_amdgcn_permlane16_swap(__float_as_uint(x), __float_as_uint(x), false, false);
  x = fmaxf(__uint_as_float(s[0]), __uint_as_float(s[1]));
  auto t = __builtin_amdgcn_permlane32_swap(__float_as_uint(x), __float_as_uint(x), false, false);
  return fmaxf(__uint_as_float(t[0]), __uint_as_float(t[1]));
}
static __device__ __forceinline__ f32x2 xrow_scatter(const float (&a)[8]) {
  const float s0 = sw32(a[0], a[4]), s1 = sw32(a[1], a[5]), s2 = sw32(a[2], a[6]), s3 = sw32(a[3], a[7]);
  return f32x2{sw16(s0, s2), sw16(s1, s3)};
}
static __device__ __forceinline__ rsrc_t mkr(const void* p, unsigned bytes) { return __builtin_amdgcn_make_buffer_rsrc((void*)p, 0, bytes, 0x00020000); }
template <int AUX> static __device__ __forceinline__ u32x4 bld(rsrc_t r, unsigned voff, unsigned soff) { return __builtin_amdgcn_raw_buffer_load_b128(r, voff, soff, AUX); }
static __device__ __forceinline__ void fma8(float (&acc)[8], float p, u32x4 v) {
#pragma unroll
  for (int k = 0; k < 4; ++k) { acc[2 * k] = fmaf(p, bflo(v[k]), acc[2 * k]); acc[2 * k + 1] = fmaf(p, bfhi(v[k]), acc[2 * k + 1]); }
}
static __device__ __forceinline__ void zip(u32x4& A, u32x4& B) {
  u32x4 lo, hi;
#pragma unroll
  for (int k = 0; k < 4; ++k) { lo[k] = __builtin_amdgcn_perm(B[k], A[k], 0x05040100u); hi[k] = __builtin_amdgcn_perm(B[k], A[k], 0x07060302u); }
  A = lo; B = hi;
}
static __device__ __forceinline__ void dot8(float (&y)[8], unsigned pr, const u32x4& lo, const u32x4& hi) {
#pragma unroll
  for (int k = 0; k < 4; ++k) { y[2 * k] = dot2(lo[k], pr, y[2 * k]); y[2 * k + 1] = dot2(hi[k], pr, y[2 * k + 1]); }
}
#define WG_BARRIER() do { asm volatile("s_waitcnt lgkmcnt(0)" ::: "memory"); __builtin_amdgcn_s_barrier(); asm volatile("" ::: "memory"); } while (0)
#define PIN8(a) asm volatile("" : "+v"(a[0]), "+v"(a[1]), "+v"(a[2]), "+v"(a[3]), "+v"(a[4]), "+v"(a[5]), "+v"(a[6]), "+v"(a[7]) :: "memory")
#define FENCE() __builtin_amdgcn_sched_barrier(0)

constexpr size_t WS_EPOCH = 0, WS_GRAN = 4096, WS_END = WS_GRAN + (size_t)NB * NH * DM * 8;
constexpr int L_XN = 0;
constexpr int L_QP = L_XN + 4 * 256;
constexpr int L_SC = L_QP + 4 * 128;
constexpr int L_ML = L_SC + 4096;
constexpr int L_HP = L_ML + 8;
constexpr int L_REC = L_HP + 4 * 64;
constexpr int L_KB = L_REC + 4 * 128;
constexpr int L_END = L_KB + 16384;
static_assert(L_KB % 4 == 0 && L_END * 4 <= 160 * 1024, "LDS");

__global__ void __launch_bounds__(512, 2)
fused_decode_kernel(const u16* __restrict__ kc, const u16* __restrict__ x, const u16* __restrict__ wq, unsigned char* __restrict__ ws,
                    const u16* __restrict__ vc, const u16* __restrict__ w1, const u16* __restrict__ w2, const u16* __restrict__ b1,
                    const u16* __restrict__ wo, const u16* __restrict__ b2, u16* __restrict__ out) {
  asm volatile("" :: "s"(kc), "s"(x), "s"(wq), "s"(ws));
  __shared__ __attribute__((aligned(16))) float lds[L_END];
  const int tid = threadIdx.x, lane = tid & 63, w = __builtin_amdgcn_readfirstlane(tid >> 6);
  const int g = lane >> 4, c = lane & 15, rw = w & 3;
  const int bh = blockIdx.x, b = bh >> 5, h = bh & 31;
  unsigned* const epoch_p = reinterpret_cast<unsigned*>(ws + WS_EPOCH) + b * 64;
  u64* const gran = reinterpret_cast<u64*>(ws + WS_GRAN);

  if (__builtin_expect(w < 4, 1)) {
    const char* kb = reinterpret_cast<const char*>(kc) + (size_t)bh * HEAD_BYTES;
    const unsigned voff = lane * 16 + rw * 4096;
    const unsigned tstart = (unsigned)__builtin_amdgcn_s_memrealtime();
    u32x4 R[3][16];
    const float* const kl = lds + L_KB + rw * 4096;
#define LOAD1(S, T, rs, so) R[S][T] = bld<NT>(rs, voff + ((T) & 3) * 1024, (so) + ((T) >> 2) * 16384)
    {
      const rsrc_t rk = mkr(kb, HEAD_BYTES);
#pragma unroll
      for (int s = 0; s < 3; ++s) {
#pragma unroll
        for (int t = 0; t < 16; ++t) { if (s * 16 + t > THR) THROTTLE(); LOAD1(s, t, rk, s * 65536); } }
      FENCE();
      const unsigned kbase = (unsigned)(size_t)(const __attribute__((address_space(3))) float*)kl;
#pragma unroll
      for (int u = 0; u < 4; ++u) {
        unsigned keep;
        asm volatile("s_mov_b32 %0, m0\n\ts_mov_b32 m0, %1\n\t"
                     "s_waitcnt vmcnt(" STR(DTHR) ")\n\tbuffer_load_dwordx4 %2, %3, %4 offen nt lds\n\t"
                     "s_waitcnt vmcnt(" STR(DTHR) ")\n\tbuffer_load_dwordx4 %2, %3, %4 offen offset:1024 nt lds\n\t"
                     "s_waitcnt vmcnt(" STR(DTHR) ")\n\tbuffer_load_dwordx4 %2, %3, %4 offen offset:2048 nt lds\n\t"
                     "s_waitcnt vmcnt(" STR(DTHR) ")\n\tbuffer_load_dwordx4 %2, %3, %4 offen offset:3072 nt lds\n\t"
                     "s_mov_b32 m0, %0"
                     : "=&s"(keep) : "s"(kbase + u * 4096), "v"(voff), "s"(rk), "s"(3 * 65536 + u * 16384) : "memory");
      }
    }
    FENCE();
    const char* vb = reinterpret_cast<const char*>(vc) + (size_t)bh * HEAD_BYTES;
    WG_BARRIER();
    u32x4 qpk;
    {
      f32x4 q0 = {0.f, 0.f, 0.f, 0.f}, q1 = {0.f, 0.f, 0.f, 0.f};
#pragma unroll
      for (int i = 0; i < 4; ++i) {
        q0 += *reinterpret_cast<const f32x4*>(lds + L_QP + i * 128 + c * 8);
        q1 += *reinterpret_cast<const f32x4*>(lds + L_QP + i * 128 + c * 8 + 4);
      }
      qpk = u32x4{cvtpk(q0[0], q0[1]), cvtpk(q0[2], q0[3]), cvtpk(q1[0], q1[1]), cvtpk(q1[2], q1[3])};
    }
    constexpr float SCALE = 0.08838834764831845f, LOG2E = 1.4426950408889634f;
    float* const sc = lds + L_SC + rw * 1024;
    float mloc = -INFINITY;
#define DOT4(D, V) { D = 0.f; _Pragma("unroll") for (int k = 0; k < 4; ++k) D = dot2((V)[k], qpk[k], D); }
#define TR_STEP(HALF, CTRL) _Pragma("unroll") for (int k = 0; k < (HALF); ++k) { \
          const float t1_ = xs[k] + dpp<CTRL>(xs[k]), t2_ = xs[k + (HALF)] + dpp<CTRL>(xs[k + (HALF)]); xs[k] = (c & (HALF)) ? t2_ : t1_; }
#define K_FINISH(N) { \
        TR_STEP(8, 0x128) TR_STEP(4, 0x141) TR_STEP(2, 0x4E) TR_STEP(1, 0xB1) \
        const float sv = rbf(rbf(xs[0]) * SCALE) * LOG2E; \
        mloc = fmaxf(mloc, sv); \
        sc[(N) * 64 + lane] = sv; }
#define K_GROUP(N, JN, PS) { \
        const rsrc_t rn = mkr((JN) < 16 ? kb : vb, HEAD_BYTES); \
        const unsigned so = ((JN) & 15) * 65536; \
        float xs[16], ys[16]; \
        const unsigned late = LATE((JN) - 1); \
        _Pragma("unroll") for (int q = 0; q < 16; q += 2) { \
          u32x4 kq[2]; \
          if (PS == 3) { _Pragma("unroll") for (int t = q; t < q + 2; ++t) kq[t - q] = *reinterpret_cast<const u32x4*>(kl + t * 256 + lane * 4); } \
          _Pragma("unroll") for (int t = q; t < q + 2; ++t) { DOT4(xs[t], R[0][t]); asm volatile("" : "+v"(xs[t]) :: "memory"); } \
          FENCE(); \
          _Pragma("unroll") for (int t = q; t < q + 2; ++t) { THROTTLE_LATE(late); LOAD1(0, t, rn, so); } \
          FENCE(); \
          if (PS == 1 || PS == 2) { _Pragma("unroll") for (int t = q; t < q + 2; ++t) { asm volatile("" : "+v"(R[PS % 3][t])); DOT4(ys[t], R[PS % 3][t]); asm volatile("" : "+v"(ys[t])); } \
            FENCE(); } \
          if (PS == 3) { _Pragma("unroll") for (int t = q; t < q + 2; ++t) { DOT4(ys[t], kq[t - q]); asm volatile("" : "+v"(ys[t])); } \
            FENCE(); } \
        } \
        K_FINISH(N); \
        if (PS) { _Pragma("unroll") for (int t = 0; t < 16; ++t) xs[t] = ys[t]; K_FINISH(PS); } \
        FENCE(); }
    K_GROUP(0, 4, 1);
    K_GROUP(4, 5, 2);
    K_GROUP(5, 6, 3);
    for (int j = 6; j < 16; ++j) K_GROUP(j, j + 1, 0);

    const float mw = xrow_max(row16_max(mloc));
    {
      float l = 0.f;
#pragma unroll
      for (int n = 0; n < 16; ++n) { const float p = __builtin_amdgcn_exp2f(sc[n * 64 + lane] - mw); l += p; sc[n * 64 + lane] = p; }
      l = xrow_sum(row16_sum(l));
      if (lane == 0) { lds[L_ML + rw] = mw; lds[L_ML + 4 + rw] = l; }
    }
    WG_BARRIER();
    float acc[8];
#pragma unroll
    for (int k = 0; k < 8; ++k) acc[k] = 0.f;
    const rsrc_t rv = mkr(vb, HEAD_BYTES);
#define V_GROUP(J, REFILL) { \
      const unsigned so = ((J) + 1) * 65536; \
      const unsigned late = REFILL ? LATE(16 + (J)) : 0u; \
      f32x4 pp[4]; \
      _Pragma("unroll") for (int q = 0; q < 4; ++q) pp[q] = *reinterpret_cast<const f32x4*>(sc + (J) * 64 + g * 16 + q * 4); \
      _Pragma("unroll") for (int q = 0; q < 16; q += 2) { \
        _Pragma("unroll") for (int t = q; t < q + 2; ++t) fma8(acc, pp[t >> 2][t & 3], R[0][t]); \
        PIN8(acc); \
        FENCE(); \
        if (REFILL) { _Pragma("unroll") for (int t = q; t < q + 2; ++t) { THROTTLE_LATE(late); LOAD1(0, t, rv, so); } } \
        FENCE(); \
      } }
    for (int j = 0; j < 15; ++j) V_GROUP(j, true);
    float fw;
    {
      const f32x4 m4 = *reinterpret_cast<const f32x4*>(lds + L_ML), l4 = *reinterpret_cast<const f32x4*>(lds + L_ML + 4);
      const float M = fmaxf(fmaxf(m4[0], m4[1]), fmaxf(m4[2], m4[3]));
      float L = 0.f;
#pragma unroll
      for (int i = 0; i < 4; ++i) L = fmaf(__builtin_amdgcn_exp2f(m4[i] - M), l4[i], L);
      fw = __builtin_amdgcn_exp2f(mw - M) / L;
      asm volatile("" : "+v"(fw));
      FENCE();
    }
    V_GROUP(15, false);
    {
      f32x2 r = xrow_scatter(acc);
      r *= fw;
      *reinterpret_cast<f32x2*>(lds + L_REC + rw * 128 + c * 8 + g * 2) = r;
    }
    WG_BARRIER();
    if (rw == 0) {
      const unsigned tag = reinterpret_cast<const unsigned*>(lds)[L_XN];
      const f32x2 xb = *reinterpret_cast<const f32x2*>(lds + L_XN + 16 + (lane & 7) * 2);
      const rsrc_t rg = mkr(gran + (size_t)bh * 512, 4096);
      __builtin_amdgcn_s_sleep(POLL_FIRST);
      u32x4 t[4];
#pragma unroll 1
      for (unsigned spin = 0; spin < (1u << 16); ++spin) {
        bool ok = true;
#pragma unroll
        for (int k = 0; k < 4; ++k) t[k] = bld<SC1>(rg, lane * 16 + k * 1024, 0);
#pragma unroll
        for (int k = 0; k < 4; ++k) ok &= t[k][1] == tag && t[k][3] == tag;
        if (__all(ok)) break;
        __builtin_amdgcn_s_sleep(POLL_SLEEP);
        asm volatile("" ::: "memory");
      }
      if (h == 0 && lane == 0) __hip_atomic_store(epoch_p, tag, __ATOMIC_RELAXED, __HIP_MEMORY_SCOPE_AGENT);
      FENCE();
      float y0 = (__uint_as_float(t[0][0]) + __uint_as_float(t[1][0])) + (__uint_as_float(t[2][0]) + __uint_as_float(t[3][0]));
      float y1 = (__uint_as_float(t[0][2]) + __uint_as_float(t[1][2])) + (__uint_as_float(t[2][2]) + __uint_as_float(t[3][2]));
      y0 += dpp<0x128>(y0); y1 += dpp<0x128>(y1);
      y0 = xrow_sum(y0); y1 = xrow_sum(y1);
      if (lane < 8) reinterpret_cast<unsigned*>(out)[(b * DM + h * 16) / 2 + lane] = cvtpk(y0 + xb[0], y1 + xb[1]);
    }
  } else {
    __builtin_amdgcn_s_setprio(3);
    const u32x4 xv = *reinterpret_cast<const u32x4*>(x + b * DM + lane * 8);
    u32x4 W[32];
    const rsrc_t rq = mkr(wq + h * DH, DM * WQ_ROW - h * DH * 2);
    const unsigned vwq = (rw * 16 + g * 4) * WQ_ROW + c * 16;
#define LOADWQ(j) { const unsigned so_ = (((j) + b) & 7) * (64 * WQ_ROW); _Pragma("unroll") for (int t_ = 0; t_ < 4; ++t_) W[4 * (j) + t_] = bld<0>(rq, vwq, so_ + t_ * WQ_ROW); }
    LOADWQ(0);
    FENCE();
    unsigned* const xnp = reinterpret_cast<unsigned*>(lds) + L_XN + rw * 256;
    {
      float xf[8];
#pragma unroll
      for (int k = 0; k < 4; ++k) { xf[2 * k] = bflo(xv[k]); xf[2 * k + 1] = bfhi(xv[k]); }
      float s = 0.f;
#pragma unroll
      for (int k = 0; k < 8; ++k) s += xf[k];
      s = xrow_sum(row16_sum(s));
      const float mean = s * (1.f / DM);
      float q = 0.f;
#pragma unroll
      for (int k = 0; k < 8; ++k) { const float d = xf[k] - mean; q = fmaf(d, d, q); }
      q = xrow_sum(row16_sum(q));
      const float mb = rbf(mean), sd = rbf(__builtin_sqrtf(rbf(rbf(q * (1.f / DM)) + 1e-5f)));
#pragma unroll
      for (int k = 0; k < 8; ++k) xf[k] = rbf(xf[k] - mb) / sd;
      *reinterpret_cast<u32x4*>(xnp + lane * 4) = u32x4{cvtpk(xf[0], xf[1]), cvtpk(xf[2], xf[3]), cvtpk(xf[4], xf[5]), cvtpk(xf[6], xf[7])};
    }
    asm volatile("s_waitcnt lgkmcnt(0)" ::: "memory");
    {
      u32x2 xr[8];
#pragma unroll
      for (int j = 0; j < 8; ++j) xr[j] = *reinterpret_cast<const u32x2*>(xnp + ((j + b) & 7) * 32 + rw * 8 + g * 2);
      float a[8];
#pragma unroll
      for (int k = 0; k < 8; ++k) a[k] = 0.f;
      asm volatile("s_waitcnt vmcnt(0)" ::: "memory");
      FENCE();
      LOADWQ(1);
#pragma unroll
      for (int j = 0; j < 8; ++j) {
        FENCE();
        zip(W[4 * j], W[4 * j + 1]); zip(W[4 * j + 2], W[4 * j + 3]);
        asm volatile("" : "+v"(W[4 * j]), "+v"(W[4 * j + 1]), "+v"(W[4 * j + 2]), "+v"(W[4 * j + 3]) :: "memory");
        FENCE();
        if (j >= 1 && j + 1 < 8) LOADWQ(j + 1);
        FENCE();
        dot8(a, xr[j][0], W[4 * j], W[4 * j + 1]); dot8(a, xr[j][1], W[4 * j + 2], W[4 * j + 3]);
      }
      *reinterpret_cast<f32x2*>(lds + L_QP + rw * 128 + c * 8 + g * 2) = xrow_scatter(a);
    }
    WG_BARRIER();
    const unsigned epoch_v = __hip_atomic_load(epoch_p, __ATOMIC_RELAXED, __HIP_MEMORY_SCOPE_AGENT);
    const unsigned tag = __builtin_amdgcn_readfirstlane(epoch_v) * 1664525u + 1013904223u;
    const int sl = (h & 7) * 4 + (h >> 3);
    const int ch0 = sl * 5 + (sl < 12 ? sl : 12), nch = sl < 12 ? 6 : 5;
    {
      const int r = lane >> 3, chl = lane & 7;
      const rsrc_t r1 = mkr(w1 + (size_t)rw * 128 * FF, 128 * FF * 2);
      const unsigned vo = r * 16 * (FF * 2) + (ch0 + (chl < nch ? chl : nch - 1)) * 16;
      u32x4 xr[2];
#pragma unroll
      for (int i = 0; i < 2; ++i) xr[i] = *reinterpret_cast<const u32x4*>(xnp + rw * 64 + r * 8 + i * 4);
      float a[8];
#pragma unroll
      for (int k = 0; k < 8; ++k) a[k] = 0.f;
#pragma unroll
      for (int i = 0; i < 8; ++i) {
        __builtin_amdgcn_s_sleep(PACE);
        W[0] = bld<0>(r1, vo, (2 * i) * (FF * 2)); W[1] = bld<0>(r1, vo, (2 * i + 1) * (FF * 2));
        zip(W[0], W[1]); dot8(a, xr[i >> 2][i & 3], W[0], W[1]);
        PIN8(a);
        FENCE();
      }
      f32x2 r2 = xrow_scatter(a);
      r2[0] += dpp<0x128>(r2[0]); r2[1] += dpp<0x128>(r2[1]);
      if ((lane & 8) == 0) *reinterpret_cast<f32x2*>(lds + L_HP + rw * 64 + chl * 8 + g * 2) = r2;
    }
    asm volatile("" ::: "memory");
    FENCE();
    u32x4 W2[12];
    unsigned b1v;
    {
      const rsrc_t r2 = mkr(w2 + (size_t)ch0 * 8 * DM, nch * 8 * DM * 2);
      const unsigned vo = g * 12 * (DM * 2) + rw * 256 + c * 16;
      b1v = __builtin_amdgcn_raw_buffer_load_b32(mkr(b1 + ch0 * 8, nch * 16), (g * 12 + c * 2) * 2, 0, 0);
#pragma unroll
      for (int i = 0; i < 6; ++i) {
        __builtin_amdgcn_s_sleep(PACE);
        W2[2 * i] = bld<0>(r2, vo + (i & 1) * 2048, (i >> 1) * 4096); W2[2 * i + 1] = bld<0>(r2, vo + (i & 1) * 2048 + 1024, (i >> 1) * 4096);
        zip(W2[2 * i], W2[2 * i + 1]); asm volatile("" : "+v"(W2[2 * i]), "+v"(W2[2 * i + 1]) :: "memory");
        FENCE();
      }
    }
    WG_BARRIER();
    if (rw == 0) {
      const float xb = bflo(x[b * DM + h * 16 + c]) + bflo(b2[h * 16 + c]);
      if (lane < 16) lds[L_XN + 16 + c] = xb;
      if (lane == 0) reinterpret_cast<unsigned*>(lds)[L_XN] = tag;
    }
    float y[8];
    {
      const int f = g * 12 + (c < 6 ? c : 5) * 2;
      f32x2 hs = {0.f, 0.f};
#pragma unroll
      for (int i = 0; i < 4; ++i) hs += *reinterpret_cast<const f32x2*>(lds + L_HP + i * 64 + f);
      const float t0 = rbf(rbf(hs[0]) + bflo(b1v)), t1 = rbf(rbf(hs[1]) + bfhi(b1v));
      const float h0 = t0 > 0.f ? t0 : 0.f, h1 = t1 > 0.f ? t1 : 0.f;
      unsigned hp = cvtpk(h0 * h0, h1 * h1);
      if (f >= nch * 8) hp = 0u;
#pragma unroll
      for (int k = 0; k < 8; ++k) y[k] = 0.f;
      dot8(y, rowbc<0>(hp), W2[0], W2[1]); dot8(y, rowbc<1>(hp), W2[2], W2[3]); dot8(y, rowbc<2>(hp), W2[4], W2[5]);
      dot8(y, rowbc<3>(hp), W2[6], W2[7]); dot8(y, rowbc<4>(hp), W2[8], W2[9]); dot8(y, rowbc<5>(hp), W2[10], W2[11]);
      PIN8(y);
    }
    {
      const rsrc_t ro = mkr(wo + (size_t)h * DH * DM, DH * DM * 2);
      const unsigned vq = g * 32 * (DM * 2) + rw * 256 + c * 16;
#pragma unroll
      for (int i = 0; i < 16; ++i) {
        __builtin_amdgcn_s_sleep(PACE);
        W[2 * i] = bld<0>(ro, vq + (i & 1) * 2048, (i >> 1) * 4096); W[2 * i + 1] = bld<0>(ro, vq + (i & 1) * 2048 + 1024, (i >> 1) * 4096);
        zip(W[2 * i], W[2 * i + 1]); asm volatile("" : "+v"(W[2 * i]), "+v"(W[2 * i + 1]) :: "memory");
        FENCE();
      }
    }
    const rsrc_t rp = mkr(gran + (size_t)(b * 32 + rw * 8) * 512, 8 * 4096);
    const unsigned gpo = (c >> 1) * 4096 + h * 128 + ((c & 1) * 4 + g) * 16;
    WG_BARRIER();
    {
      f32x2 o2 = *reinterpret_cast<const f32x2*>(lds + L_REC + lane * 2);
#pragma unroll
      for (int i = 1; i < 4; ++i) o2 += *reinterpret_cast<const f32x2*>(lds + L_REC + i * 128 + lane * 2);
      const unsigned op = cvtpk(o2[0], o2[1]);
#define PRJ(i) dot8(y, rowbc<i>(op), W[2 * (i)], W[2 * (i) + 1]);
      PRJ(0) PRJ(1) PRJ(2) PRJ(3) PRJ(4) PRJ(5) PRJ(6) PRJ(7) PRJ(8) PRJ(9) PRJ(10) PRJ(11) PRJ(12) PRJ(13) PRJ(14) PRJ(15)
#undef PRJ
      const f32x2 r = xrow_scatter(y);
      __builtin_amdgcn_raw_buffer_store_b128(u32x4{__float_as_uint(r[0]), tag, __float_as_uint(r[1]), tag}, rp, gpo, 0, SC1);
    }
  }
}

extern "C" void kernel_launch(void* const* d_in, const int* in_sizes, int n_in, void* d_out, int out_size, void* d_ws, size_t ws_size, hipStream_t stream) {
  if (n_in != 10 || out_size != NB * DM || ws_size < WS_END || in_sizes[1] != NB * NH * SK * DH || in_sizes[6] != DM * FF) return;
  fused_decode_kernel<<<NB * NH, 512, 0, stream>>>((const u16*)d_in[1], (const u16*)d_in[0], (const u16*)d_in[3], (unsigned char*)d_ws, (const u16*)d_in[2],
                                                   (const u16*)d_in[6], (const u16*)d_in[8], (const u16*)d_in[7], (const u16*)d_in[4], (const u16*)d_in[9], (u16*)d_out);
}
```

```cpp
#define DIAG 0
#include <hip/hip_runtime.h>
#include <hip/hip_bf16.h>
#include <stdint.h>

typedef unsigned u32x4 __attribute__((ext_vector_type(4)));
typedef unsigned u32x2 __attribute__((ext_vector_type(2)));
typedef float f32x4 __attribute__((ext_vector_type(4)));
typedef float f32x2 __attribute__((ext_vector_type(2)));
typedef __bf16 bf16x2 __attribute__((ext_vector_type(2)));
typedef unsigned short u16;
typedef unsigned long long u64;
typedef __amdgpu_buffer_rsrc_t rsrc_t;

constexpr int NB = 8, NH = 32, SK = 4096, DH = 128, DM = 512, FF = 1376;
constexpr unsigned HEAD_BYTES = SK * DH * 2;
constexpr unsigned WQ_ROW = NH * DH * 2;

#define THR 5
#define HTHR 7
#define SCH_A 690
#define SCH_B 214
#define STR2(x) #x
#define STR(x) STR2(x)
#define THROTTLE() asm volatile("s_waitcnt vmcnt(" STR(THR) ")" ::: "memory")
#define THROTTLE_LATE(late) asm volatile("s_waitcnt vmcnt(" STR(HTHR) ")\n\ts_cmp_gt_i32 %0, 0\n\ts_cbranch_scc1 1f\n\ts_waitcnt vmcnt(" STR(THR) ")\n1:" :: "s"(late) : "memory", "scc")
#define LATE(n) ((unsigned)__builtin_amdgcn_s_memrealtime() - tstart - (unsigned)(SCH_A + SCH_B * (n)))
#define DTHR 6
constexpr int PACE = 16, POLL_FIRST = 32, POLL_SLEEP = 1;
constexpr int SC1 = 16, NT = 2;

static __device__ __forceinline__ float dot2(unsigned a, unsigned b, float c) {
  return __builtin_amdgcn_fdot2_f32_bf16(__builtin_bit_cast(bf16x2, a), __builtin_bit_cast(bf16x2, b), c, false);
}
static __device__ __forceinline__ float bflo(unsigned u) { return __builtin_bit_cast(float, u << 16); }
static __device__ __forceinline__ float bfhi(unsigned u) { return __builtin_bit_cast(float, u & 0xffff0000u); }
static __device__ __forceinline__ unsigned cvtpk(float lo, float hi) {
  bf16x2 r = {(__bf16)lo, (__bf16)hi};
  return __builtin_bit_cast(unsigned, r);
}
static __device__ __forceinline__ float rbf(float x) { return (float)(__bf16)x; }
template <int CTRL> static __device__ __forceinline__ float dpp(float x) {
  return __builtin_bit_cast(float, __builtin_amdgcn_mov_dpp(__builtin_bit_cast(int, x), CTRL, 0xf, 0xf, true));
}
template <int N> static __device__ __forceinline__ unsigned rowbc(unsigned x) {
  return (unsigned)__builtin_amdgcn_mov_dpp((int)x, 0x150 + N, 0xf, 0xf, true);
}
static __device__ __forceinline__ float row16_sum(float x) { x += dpp<0x128>(x); x += dpp<0x124>(x); x += dpp<0x122>(x); x += dpp<0x121>(x); return x; }
static __device__ __forceinline__ float row16_max(float x) { x = fmaxf(x, dpp<0x128>(x)); x = fmaxf(x, dpp<0x124>(x)); x = fmaxf(x, dpp<0x122>(x)); x = fmaxf(x, dpp<0x121>(x)); return x; }
static __device__ __forceinline__ float sw32(float a, float b) {
  auto t = __builtin_amdgcn_permlane32_swap(__float_as_uint(a), __float_as_uint(b), false, false);
  return __uint_as_float(t[0]) + __uint_as_float(t[1]);
}
static __device__ __forceinline__ float sw16(float a, float b) {
  auto t = __builtin_amdgcn_permlane16_swap(__float_as_uint(a), __float_as_uint(b), false, false);
  return __uint_as_float(t[0]) + __uint_as_float(t[1]);
}
static __device__ __forceinline__ float xrow_sum(float x) { x = sw16(x, x); return sw32(x, x); }
static __device__ __forceinline__ float xrow_max(float x) {
  auto s = __builtin_amdgcn_permlane16_swap(__float_as_uint(x), __float_as_uint(x), false, false);
  x = fmaxf(__uint_as_float(s[0]), __uint_as_float(s[1]));
  auto t = __builtin_amdgcn_permlane32_swap(__float_as_uint(x), __float_as_uint(x), false, false);
  return fmaxf(__uint_as_float(t[0]), __uint_as_float(t[1]));
}
static __device__ __forceinline__ f32x2 xrow_scatter(const float (&a)[8]) {
  const float s0 = sw32(a[0], a[4]), s1 = sw32(a[1], a[5]), s2 = sw32(a[2], a[6]), s3 = sw32(a[3], a[7]);
  return f32x2{sw16(s0, s2), sw16(s1, s3)};
}
static __device__ __forceinline__ rsrc_t mkr(const void* p, unsigned bytes) { return __builtin_amdgcn_make_buffer_rsrc((void*)p, 0, bytes, 0x00020000); }
template <int AUX> static __device__ __forceinline__ u32x4 bld(rsrc_t r, unsigned voff, unsigned soff) { return __builtin_amdgcn_raw_buffer_load_b128(r, voff, soff, AUX); }
static __device__ __forceinline__ void fma8(float (&acc)[8], float p, u32x4 v) {
#pragma unroll
  for (int k = 0; k < 4; ++k) { acc[2 * k] = fmaf(p, bflo(v[k]), acc[2 * k]); acc[2 * k + 1] = fmaf(p, bfhi(v[k]), acc[2 * k + 1]); }
}
static __device__ __forceinline__ void zip(u32x4& A, u32x4& B) {
  u32x4 lo, hi;
#pragma unroll
  for (int k = 0; k < 4; ++k) { lo[k] = __builtin_amdgcn_perm(B[k], A[k], 0x05040100u); hi[k] = __builtin_amdgcn_perm(B[k], A[k], 0x07060302u); }
  A = lo; B = hi;
}
static __device__ __forceinline__ void dot8(float (&y)[8], unsigned pr, const u32x4& lo, const u32x4& hi) {
#pragma unroll
  for (int k = 0; k < 4; ++k) { y[2 * k] = dot2(lo[k], pr, y[2 * k]); y[2 * k + 1] = dot2(hi[k], pr, y[2 * k + 1]); }
}
#define WG_BARRIER() do { asm volatile("s_waitcnt lgkmcnt(0)" ::: "memory"); __builtin_amdgcn_s_barrier(); asm volatile("" ::: "memory"); } while (0)
#define PIN8(a) asm volatile("" : "+v"(a[0]), "+v"(a[1]), "+v"(a[2]), "+v"(a[3]), "+v"(a[4]), "+v"(a[5]), "+v"(a[6]), "+v"(a[7]) :: "memory")
#define FENCE() __builtin_amdgcn_sched_barrier(0)

constexpr size_t WS_EPOCH = 0, WS_GRAN = 4096, WS_END = WS_GRAN + (size_t)NB * NH * DM * 8;
constexpr int L_XN = 0;
constexpr int L_QP = L_XN + 4 * 256;
constexpr int L_SC = L_QP + 4 * 128;
constexpr int L_ML = L_SC + 4096;
constexpr int L_HP = L_ML + 8;
constexpr int L_REC = L_HP + 4 * 64;
constexpr int L_KB = L_REC + 4 * 128;
constexpr int L_END = L_KB + 16384;
static_assert(L_KB % 4 == 0 && L_END * 4 <= 160 * 1024, "LDS");

__global__ void __launch_bounds__(512, 2)
fused_decode_kernel(const u16* __restrict__ kc, const u16* __restrict__ x, const u16* __restrict__ wq, unsigned char* __restrict__ ws,
                    const u16* __restrict__ vc, const u16* __restrict__ w1, const u16* __restrict__ w2, const u16* __restrict__ b1,
                    const u16* __restrict__ wo, const u16* __restrict__ b2, u16* __restrict__ out) {
  const unsigned tfirst = (unsigned)__builtin_amdgcn_s_memrealtime();
  __builtin_amdgcn_sched_barrier(0);
  asm volatile("" :: "s"(kc), "s"(x), "s"(wq), "s"(ws));
  __shared__ __attribute__((aligned(16))) float lds[L_END];
  const int tid = threadIdx.x, lane = tid & 63, w = __builtin_amdgcn_readfirstlane(tid >> 6);
  const int g = lane >> 4, c = lane & 15, rw = w & 3;
  const int bh = blockIdx.x, b = bh >> 5, h = bh & 31;
  unsigned* const epoch_p = reinterpret_cast<unsigned*>(ws + WS_EPOCH) + b * 64;
  u64* const gran = reinterpret_cast<u64*>(ws + WS_GRAN);

  if (__builtin_expect(w < 4, 1)) {
    const char* kb = reinterpret_cast<const char*>(kc) + (size_t)bh * HEAD_BYTES;
    const unsigned voff = lane * 16 + rw * 4096;
    const unsigned tstart = (unsigned)__builtin_amdgcn_s_memrealtime();
    u32x4 R[3][16];
    const float* const kl = lds + L_KB + rw * 4096;
#define LOAD1(S, T, rs, so) R[S][T] = bld<NT>(rs, voff + ((T) & 3) * 1024, (so) + ((T) >> 2) * 16384)
    {
      const rsrc_t rk = mkr(kb, HEAD_BYTES);
#pragma unroll
      for (int s = 0; s < 3; ++s) {
#pragma unroll
        for (int t = 0; t < 16; ++t) { if (s * 16 + t > THR) THROTTLE(); LOAD1(s, t, rk, s * 65536); } }
      FENCE();
      const unsigned kbase = (unsigned)(size_t)(const __attribute__((address_space(3))) float*)kl;
#pragma unroll
      for (int u = 0; u < 4; ++u) {
        unsigned keep;
        asm volatile("s_mov_b32 %0, m0\n\ts_mov_b32 m0, %1\n\t"
                     "s_waitcnt vmcnt(" STR(DTHR) ")\n\tbuffer_load_dwordx4 %2, %3, %4 offen nt lds\n\t"
                     "s_waitcnt vmcnt(" STR(DTHR) ")\n\tbuffer_load_dwordx4 %2, %3, %4 offen offset:1024 nt lds\n\t"
                     "s_waitcnt vmcnt(" STR(DTHR) ")\n\tbuffer_load_dwordx4 %2, %3, %4 offen offset:2048 nt lds\n\t"
                     "s_waitcnt vmcnt(" STR(DTHR) ")\n\tbuffer_load_dwordx4 %2, %3, %4 offen offset:3072 nt lds\n\t"
                     "s_mov_b32 m0, %0"
                     : "=&s"(keep) : "s"(kbase + u * 4096), "v"(voff), "s"(rk), "s"(3 * 65536 + u * 16384) : "memory");
      }
    }
    FENCE();
    const char* vb = reinterpret_cast<const char*>(vc) + (size_t)bh * HEAD_BYTES;
    WG_BARRIER();
    u32x4 qpk;
    {
      f32x4 q0 = {0.f, 0.f, 0.f, 0.f}, q1 = {0.f, 0.f, 0.f, 0.f};
#pragma unroll
      for (int i = 0; i < 4; ++i) {
        q0 += *reinterpret_cast<const f32x4*>(lds + L_QP + i * 128 + c * 8);
        q1 += *reinterpret_cast<const f32x4*>(lds + L_QP + i * 128 + c * 8 + 4);
      }
      qpk = u32x4{cvtpk(q0[0], q0[1]), cvtpk(q0[2], q0[3]), cvtpk(q1[0], q1[1]), cvtpk(q1[2], q1[3])};
    }
    constexpr float SCALE = 0.08838834764831845f, LOG2E = 1.4426950408889634f;
    float* const sc = lds + L_SC + rw * 1024;
    float mloc = -INFINITY;
#define DOT4(D, V) { D = 0.f; _Pragma("unroll") for (int k = 0; k < 4; ++k) D = dot2((V)[k], qpk[k], D); }
#define TR_STEP(HALF, CTRL) _Pragma("unroll") for (int k = 0; k < (HALF); ++k) { \
          const float t1_ = xs[k] + dpp<CTRL>(xs[k]), t2_ = xs[k + (HALF)] + dpp<CTRL>(xs[k + (HALF)]); xs[k] = (c & (HALF)) ? t2_ : t1_; }
#define K_FINISH(N) { \
        TR_STEP(8, 0x128) TR_STEP(4, 0x141) TR_STEP(2, 0x4E) TR_STEP(1, 0xB1) \
        const float sv = rbf(rbf(xs[0]) * SCALE) * LOG2E; \
        mloc = fmaxf(mloc, sv); \
        sc[(N) * 64 + lane] = sv; }
#define K_GROUP(N, JN, PS) { \
        const rsrc_t rn = mkr((JN) < 16 ? kb : vb, HEAD_BYTES); \
        const unsigned so = ((JN) & 15) * 65536; \
        float xs[16], ys[16]; \
        const unsigned late = LATE((JN) - 1); \
        _Pragma("unroll") for (int q = 0; q < 16; q += 2) { \
          u32x4 kq[2]; \
          if (PS == 3) { _Pragma("unroll") for (int t = q; t < q + 2; ++t) kq[t - q] = *reinterpret_cast<const u32x4*>(kl + t * 256 + lane * 4); } \
          _Pragma("unroll") for (int t = q; t < q + 2; ++t) { DOT4(xs[t], R[0][t]); asm volatile("" : "+v"(xs[t]) :: "memory"); } \
          FENCE(); \
          _Pragma("unroll") for (int t = q; t < q + 2; ++t) { THROTTLE_LATE(late); LOAD1(0, t, rn, so); } \
          FENCE(); \
          if (PS == 1 || PS == 2) { _Pragma("unroll") for (int t = q; t < q + 2; ++t) { asm volatile("" : "+v"(R[PS % 3][t])); DOT4(ys[t], R[PS % 3][t]); asm volatile("" : "+v"(ys[t])); } \
            FENCE(); } \
          if (PS == 3) { _Pragma("unroll") for (int t = q; t < q + 2; ++t) { DOT4(ys[t], kq[t - q]); asm volatile("" : "+v"(ys[t])); } \
            FENCE(); } \
        } \
        K_FINISH(N); \
        if (PS) { _Pragma("unroll") for (int t = 0; t < 16; ++t) xs[t] = ys[t]; K_FINISH(PS); } \
        FENCE(); }
    K_GROUP(0, 4, 1);
    K_GROUP(4, 5, 2);
    K_GROUP(5, 6, 3);
    for (int j = 6; j < 16; ++j) K_GROUP(j, j + 1, 0);

    const float mw = xrow_max(row16_max(mloc));
    {
      float l = 0.f;
#pragma unroll
      for (int n = 0; n < 16; ++n) { const float p = __builtin_amdgcn_exp2f(sc[n * 64 + lane] - mw); l += p; sc[n * 64 + lane] = p; }
      l = xrow_sum(row16_sum(l));
      if (lane == 0) { lds[L_ML + rw] = mw; lds[L_ML + 4 + rw] = l; }
    }
    WG_BARRIER();
    float acc[8];
#pragma unroll
    for (int k = 0; k < 8; ++k) acc[k] = 0.f;
    const rsrc_t rv = mkr(vb, HEAD_BYTES);
#define V_GROUP(J, REFILL) { \
      const unsigned so = ((J) + 1) * 65536; \
      const unsigned late = REFILL ? LATE(16 + (J)) : 0u; \
      f32x4 pp[4]; \
      _Pragma("unroll") for (int q = 0; q < 4; ++q) pp[q] = *reinterpret_cast<const f32x4*>(sc + (J) * 64 + g * 16 + q * 4); \
      _Pragma("unroll") for (int q = 0; q < 16; q += 2) { \
        _Pragma("unroll") for (int t = q; t < q + 2; ++t) fma8(acc, pp[t >> 2][t & 3], R[0][t]); \
        PIN8(acc); \
        FENCE(); \
        if (REFILL) { _Pragma("unroll") for (int t = q; t < q + 2; ++t) { THROTTLE_LATE(late); LOAD1(0, t, rv, so); } } \
        FENCE(); \
      } }
    for (int j = 0; j < 15; ++j) V_GROUP(j, true);
    float fw;
    {
      const f32x4 m4 = *reinterpret_cast<const f32x4*>(lds + L_ML), l4 = *reinterpret_cast<const f32x4*>(lds + L_ML + 4);
      const float M = fmaxf(fmaxf(m4[0], m4[1]), fmaxf(m4[2], m4[3]));
      float L = 0.f;
#pragma unroll
      for (int i = 0; i < 4; ++i) L = fmaf(__builtin_amdgcn_exp2f(m4[i] - M), l4[i], L);
      fw = __builtin_amdgcn_exp2f(mw - M) / L;
      asm volatile("" : "+v"(fw));
      FENCE();
    }
    if (rw == 0 && lane == 0) __hip_atomic_store(reinterpret_cast<unsigned*>(ws + (8u << 20)) + bh, DIAG == 1 ? tstart : tfirst, __ATOMIC_RELAXED, __HIP_MEMORY_SCOPE_AGENT);
    V_GROUP(15, false);
    {
      f32x2 r = xrow_scatter(acc);
      r *= fw;
      *reinterpret_cast<f32x2*>(lds + L_REC + rw * 128 + c * 8 + g * 2) = r;
    }
    WG_BARRIER();
    if (rw == 0) {
      const unsigned tag = reinterpret_cast<const unsigned*>(lds)[L_XN];
      const f32x2 xb = *reinterpret_cast<const f32x2*>(lds + L_XN + 16 + (lane & 7) * 2);
      const rsrc_t rg = mkr(gran + (size_t)bh * 512, 4096);
      __builtin_amdgcn_s_sleep(POLL_FIRST);
      u32x4 t[4];
#pragma unroll 1
      for (unsigned spin = 0; spin < (1u << 16); ++spin) {
        bool ok = true;
#pragma unroll
        for (int k = 0; k < 4; ++k) t[k] = bld<SC1>(rg, lane * 16 + k * 1024, 0);
#pragma unroll
        for (int k = 0; k < 4; ++k) ok &= t[k][1] == tag && t[k][3] == tag;
        if (__all(ok)) break;
        __builtin_amdgcn_s_sleep(POLL_SLEEP);
        asm volatile("" ::: "memory");
      }
      if (h == 0 && lane == 0) __hip_atomic_store(epoch_p, tag, __ATOMIC_RELAXED, __HIP_MEMORY_SCOPE_AGENT);
      FENCE();
      float y0 = (__uint_as_float(t[0][0]) + __uint_as_float(t[1][0])) + (__uint_as_float(t[2][0]) + __uint_as_float(t[3][0]));
      float y1 = (__uint_as_float(t[0][2]) + __uint_as_float(t[1][2])) + (__uint_as_float(t[2][2]) + __uint_as_float(t[3][2]));
      y0 += dpp<0x128>(y0); y1 += dpp<0x128>(y1);
      y0 = xrow_sum(y0); y1 = xrow_sum(y1);
      if (lane < 8) reinterpret_cast<unsigned*>(out)[(b * DM + h * 16) / 2 + lane] = cvtpk(y0 + xb[0], y1 + xb[1]);
      if (DIAG) {
        const unsigned o = __hip_atomic_load(reinterpret_cast<unsigned*>(ws + (8u << 20)) + b * 32 + (lane & 31), __ATOMIC_RELAXED, __HIP_MEMORY_SCOPE_AGENT);
        const float d = (float)(int)(o - tstart);
        const float mx = xrow_max(row16_max(d)), mn = -xrow_max(row16_max(-d));
        unsigned ex = __builtin_amdgcn_readfirstlane((unsigned)(mx - mn)) * 16u; if (ex > 20000u) ex = 20000u;
        const unsigned te = (unsigned)__builtin_amdgcn_s_memrealtime();
        while ((unsigned)__builtin_amdgcn_s_memrealtime() - te < ex) __builtin_amdgcn_s_sleep(1);
      }
    }
  } else {
    __builtin_amdgcn_s_setprio(3);
    const u32x4 xv = *reinterpret_cast<const u32x4*>(x + b * DM + lane * 8);
    u32x4 W[32];
    const rsrc_t rq = mkr(wq + h * DH, DM * WQ_ROW - h * DH * 2);
    const unsigned vwq = (rw * 16 + g * 4) * WQ_ROW + c * 16;
#define LOADWQ(j) { const unsigned so_ = (((j) + b) & 7) * (64 * WQ_ROW); _Pragma("unroll") for (int t_ = 0; t_ < 4; ++t_) W[4 * (j) + t_] = bld<0>(rq, vwq, so_ + t_ * WQ_ROW); }
    LOADWQ(0);
    FENCE();
    unsigned* const xnp = reinterpret_cast<unsigned*>(lds) + L_XN + rw * 256;
    {
      float xf[8];
#pragma unroll
      for (int k = 0; k < 4; ++k) { xf[2 * k] = bflo(xv[k]); xf[2 * k + 1] = bfhi(xv[k]); }
      float s = 0.f;
#pragma unroll
      for (int k = 0; k < 8; ++k) s += xf[k];
      s = xrow_sum(row16_sum(s));
      const float mean = s * (1.f / DM);
      float q = 0.f;
#pragma unroll
      for (int k = 0; k < 8; ++k) { const float d = xf[k] - mean; q = fmaf(d, d, q); }
      q = xrow_sum(row16_sum(q));
      const float mb = rbf(mean), sd = rbf(__builtin_sqrtf(rbf(rbf(q * (1.f / DM)) + 1e-5f)));
#pragma unroll
      for (int k = 0; k < 8; ++k) xf[k] = rbf(xf[k] - mb) / sd;
      *reinterpret_cast<u32x4*>(xnp + lane * 4) = u32x4{cvtpk(xf[0], xf[1]), cvtpk(xf[2], xf[3]), cvtpk(xf[4], xf[5]), cvtpk(xf[6], xf[7])};
    }
    asm volatile("s_waitcnt lgkmcnt(0)" ::: "memory");
    {
      u32x2 xr[8];
#pragma unroll
      for (int j = 0; j < 8; ++j) xr[j] = *reinterpret_cast<const u32x2*>(xnp + ((j + b) & 7) * 32 + rw * 8 + g * 2);
      float a[8];
#pragma unroll
      for (int k = 0; k < 8; ++k) a[k] = 0.f;
      asm volatile("s_waitcnt vmcnt(0)" ::: "memory");
      FENCE();
      LOADWQ(1);
#pragma unroll
      for (int j = 0; j < 8; ++j) {
        FENCE();
        zip(W[4 * j], W[4 * j + 1]); zip(W[4 * j + 2], W[4 * j + 3]);
        asm volatile("" : "+v"(W[4 * j]), "+v"(W[4 * j + 1]), "+v"(W[4 * j + 2]), "+v"(W[4 * j + 3]) :: "memory");
        FENCE();
        if (j >= 1 && j + 1 < 8) LOADWQ(j + 1);
        FENCE();
        dot8(a, xr[j][0], W[4 * j], W[4 * j + 1]); dot8(a, xr[j][1], W[4 * j + 2], W[4 * j + 3]);
      }
      *reinterpret_cast<f32x2*>(lds + L_QP + rw * 128 + c * 8 + g * 2) = xrow_scatter(a);
    }
    WG_BARRIER();
    const unsigned epoch_v = __hip_atomic_load(epoch_p, __ATOMIC_RELAXED, __HIP_MEMORY_SCOPE_AGENT);
    const unsigned tag = __builtin_amdgcn_readfirstlane(epoch_v) * 1664525u + 1013904223u;
    const int sl = (h & 7) * 4 + (h >> 3);
    const int ch0 = sl * 5 + (sl < 12 ? sl : 12), nch = sl < 12 ? 6 : 5;
    {
      const int r = lane >> 3, chl = lane & 7;
      const rsrc_t r1 = mkr(w1 + (size_t)rw * 128 * FF, 128 * FF * 2);
      const unsigned vo = r * 16 * (FF * 2) + (ch0 + (chl < nch ? chl : nch - 1)) * 16;
      u32x4 xr[2];
#pragma unroll
      for (int i = 0; i < 2; ++i) xr[i] = *reinterpret_cast<const u32x4*>(xnp + rw * 64 + r * 8 + i * 4);
      float a[8];
#pragma unroll
      for (int k = 0; k < 8; ++k) a[k] = 0.f;
#pragma unroll
      for (int i = 0; i < 8; ++i) {
        __builtin_amdgcn_s_sleep(PACE);
        W[0] = bld<0>(r1, vo, (2 * i) * (FF * 2)); W[1] = bld<0>(r1, vo, (2 * i + 1) * (FF * 2));
        zip(W[0], W[1]); dot8(a, xr[i >> 2][i & 3], W[0], W[1]);
        PIN8(a);
        FENCE();
      }
      f32x2 r2 = xrow_scatter(a);
      r2[0] += dpp<0x128>(r2[0]); r2[1] += dpp<0x128>(r2[1]);
      if ((lane & 8) == 0) *reinterpret_cast<f32x2*>(lds + L_HP + rw * 64 + chl * 8 + g * 2) = r2;
    }
    asm volatile("" ::: "memory");
    FENCE();
    u32x4 W2[12];
    unsigned b1v;
    {
      const rsrc_t r2 = mkr(w2 + (size_t)ch0 * 8 * DM, nch * 8 * DM * 2);
      const unsigned vo = g * 12 * (DM * 2) + rw * 256 + c * 16;
      b1v = __builtin_amdgcn_raw_buffer_load_b32(mkr(b1 + ch0 * 8, nch * 16), (g * 12 + c * 2) * 2, 0, 0);
#pragma unroll
      for (int i = 0; i < 6; ++i) {
        __builtin_amdgcn_s_sleep(PACE);
        W2[2 * i] = bld<0>(r2, vo + (i & 1) * 2048, (i >> 1) * 4096); W2[2 * i + 1] = bld<0>(r2, vo + (i & 1) * 2048 + 1024, (i >> 1) * 4096);
        zip(W2[2 * i], W2[2 * i + 1]); asm volatile("" : "+v"(W2[2 * i]), "+v"(W2[2 * i + 1]) :: "memory");
        FENCE();
      }
    }
    WG_BARRIER();
    if (rw == 0) {
      const float xb = bflo(x[b * DM + h * 16 + c]) + bflo(b2[h * 16 + c]);
      if (lane < 16) lds[L_XN + 16 + c] = xb;
      if (lane == 0) reinterpret_cast<unsigned*>(lds)[L_XN] = tag;
    }
    float y[8];
    {
      const int f = g * 12 + (c < 6 ? c : 5) * 2;
      f32x2 hs = {0.f, 0.f};
#pragma unroll
      for (int i = 0; i < 4; ++i) hs += *reinterpret_cast<const f32x2*>(lds + L_HP + i * 64 + f);
      const float t0 = rbf(rbf(hs[0]) + bflo(b1v)), t1 = rbf(rbf(hs[1]) + bfhi(b1v));
      const float h0 = t0 > 0.f ? t0 : 0.f, h1 = t1 > 0.f ? t1 : 0.f;
      unsigned hp = cvtpk(h0 * h0, h1 * h1);
      if (f >= nch * 8) hp = 0u;
#pragma unroll
      for (int k = 0; k < 8; ++k) y[k] = 0.f;
      dot8(y, rowbc<0>(hp), W2[0], W2[1]); dot8(y, rowbc<1>(hp), W2[2], W2[3]); dot8(y, rowbc<2>(hp), W2[4], W2[5]);
      dot8(y, rowbc<3>(hp), W2[6], W2[7]); dot8(y, rowbc<4>(hp), W2[8], W2[9]); dot8(y, rowbc<5>(hp), W2[10], W2[11]);
      PIN8(y);
    }
    {
      const rsrc_t ro = mkr(wo + (size_t)h * DH * DM, DH * DM * 2);
      const unsigned vq = g * 32 * (DM * 2) + rw * 256 + c * 16;
#pragma unroll
      for (int i = 0; i < 16; ++i) {
        __builtin_amdgcn_s_sleep(PACE);
        W[2 * i] = bld<0>(ro, vq + (i & 1) * 2048, (i >> 1) * 4096); W[2 * i + 1] = bld<0>(ro, vq + (i & 1) * 2048 + 1024, (i >> 1) * 4096);
        zip(W[2 * i], W[2 * i + 1]); asm volatile("" : "+v"(W[2 * i]), "+v"(W[2 * i + 1]) :: "memory");
        FENCE();
      }
    }
    const rsrc_t rp = mkr(gran + (size_t)(b * 32 + rw * 8) * 512, 8 * 4096);
    const unsigned gpo = (c >> 1) * 4096 + h * 128 + ((c & 1) * 4 + g) * 16;
    WG_BARRIER();
    {
      f32x2 o2 = *reinterpret_cast<const f32x2*>(lds + L_REC + lane * 2);
#pragma unroll
      for (int i = 1; i < 4; ++i) o2 += *reinterpret_cast<const f32x2*>(lds + L_REC + i * 128 + lane * 2);
      const unsigned op = cvtpk(o2[0], o2[1]);
#define PRJ(i) dot8(y, rowbc<i>(op), W[2 * (i)], W[2 * (i) + 1]);
      PRJ(0) PRJ(1) PRJ(2) PRJ(3) PRJ(4) PRJ(5) PRJ(6) PRJ(7) PRJ(8) PRJ(9) PRJ(10) PRJ(11) PRJ(12) PRJ(13) PRJ(14) PRJ(15)
#undef PRJ
      const f32x2 r = xrow_scatter(y);
      __builtin_amdgcn_raw_buffer_store_b128(u32x4{__float_as_uint(r[0]), tag, __float_as_uint(r[1]), tag}, rp, gpo, 0, SC1);
    }
  }
}

extern "C" void kernel_launch(void* const* d_in, const int* in_sizes, int n_in, void* d_out, int out_size, void* d_ws, size_t ws_size, hipStream_t stream) {
  if (n_in != 10 || out_size != NB * DM || ws_size < WS_END || in_sizes[1] != NB * NH * SK * DH || in_sizes[6] != DM * FF) return;
  fused_decode_kernel<<<NB * NH, 512, 0, stream>>>((const u16*)d_in[1], (const u16*)d_in[0], (const u16*)d_in[3], (unsigned char*)d_ws, (const u16*)d_in[2],
                                                   (const u16*)d_in[6], (const u16*)d_in[8], (const u16*)d_in[7], (const u16*)d_in[4], (const u16*)d_in[9], (u16*)d_out);
}
```

```cpp
#include <hip/hip_runtime.h>
#include <hip/hip_bf16.h>
#include <stdint.h>

typedef unsigned u32x4 __attribute__((ext_vector_type(4)));
typedef unsigned u32x2 __attribute__((ext_vector_type(2)));
typedef float f32x4 __attribute__((ext_vector_type(4)));
typedef float f32x2 __attribute__((ext_vector_type(2)));
typedef __bf16 bf16x2 __attribute__((ext_vector_type(2)));
typedef unsigned short u16;
typedef unsigned long long u64;
typedef __amdgpu_buffer_rsrc_t rsrc_t;

constexpr int NB = 8, NH = 32, SK = 4096, DH = 128, DM = 512, FF = 1376;
constexpr unsigned HEAD_BYTES = SK * DH * 2;
constexpr unsigned WQ_ROW = NH * DH * 2;

#define THR 5
#define THR_LO 4
#define THR_HI 7
#define THR_LDS 6
#define SCH_A 380
#define SCH_B 218
#define SCH_LAG 240
#define STR2(x) #x
#define STR(x) STR2(x)
#define THROTTLE() asm volatile("s_waitcnt vmcnt(" STR(THR) ")" ::: "memory")
#define THROTTLE_LATE(late) asm volatile("s_waitcnt vmcnt(" STR(THR_HI) ")\n\ts_cmp_gt_i32 %0, " STR(SCH_LAG) "\n\ts_cbranch_scc1 1f\n\t" \
                                         "s_waitcnt vmcnt(" STR(THR) ")\n\ts_cmp_gt_i32 %0, 0\n\ts_cbranch_scc1 1f\n\t" \
                                         "s_waitcnt vmcnt(" STR(THR_LO) ")\n1:" :: "s"(late) : "memory", "scc")
#define LATE(n) ((unsigned)__builtin_amdgcn_s_memrealtime() - tstart - (unsigned)(SCH_A + SCH_B * (n)))
constexpr int PACE = 16, POLL_FIRST = 32, POLL_SLEEP = 1;
constexpr int SC1 = 16, NT = 2;

static __device__ __forceinline__ float dot2(unsigned a, unsigned b, float c) {
  return __builtin_amdgcn_fdot2_f32_bf16(__builtin_bit_cast(bf16x2, a), __builtin_bit_cast(bf16x2, b), c, false);
}
static __device__ __forceinline__ float bflo(unsigned u) { return __builtin_bit_cast(float, u << 16); }
static __device__ __forceinline__ float bfhi(unsigned u) { return __builtin_bit_cast(float, u & 0xffff0000u); }
static __device__ __forceinline__ unsigned cvtpk(float lo, float hi) {
  bf16x2 r = {(__bf16)lo, (__bf16)hi};
  return __builtin_bit_cast(unsigned, r);
}
static __device__ __forceinline__ float rbf(float x) { return (float)(__bf16)x; }
template <int CTRL> static __device__ __forceinline__ float dpp(float x) {
  return __builtin_bit_cast(float, __builtin_amdgcn_mov_dpp(__builtin_bit_cast(int, x), CTRL, 0xf, 0xf, true));
}
template <int N> static __device__ __forceinline__ unsigned rowbc(unsigned x) {
  return (unsigned)__builtin_amdgcn_mov_dpp((int)x, 0x150 + N, 0xf, 0xf, true);
}
static __device__ __forceinline__ float row16_sum(float x) { x += dpp<0x128>(x); x += dpp<0x124>(x); x += dpp<0x122>(x); x += dpp<0x121>(x); return x; }
static __device__ __forceinline__ float row16_max(float x) { x = fmaxf(x, dpp<0x128>(x)); x = fmaxf(x, dpp<0x124>(x)); x = fmaxf(x, dpp<0x122>(x)); x = fmaxf(x, dpp<0x121>(x)); return x; }
static __device__ __forceinline__ float sw32(float a, float b) {
  auto t = __builtin_amdgcn_permlane32_swap(__float_as_uint(a), __float_as_uint(b), false, false);
  return __uint_as_float(t[0]) + __uint_as_float(t[1]);
}
static __device__ __forceinline__ float sw16(float a, float b) {
  auto t = __builtin_amdgcn_permlane16_swap(__float_as_uint(a), __float_as_uint(b), false, false);
  return __uint_as_float(t[0]) + __uint_as_float(t[1]);
}
static __device__ __forceinline__ float xrow_sum(float x) { x = sw16(x, x); return sw32(x, x); }
static __device__ __forceinline__ float xrow_max(float x) {
  auto s = __builtin_amdgcn_permlane16_swap(__float_as_uint(x), __float_as_uint(x), false, false);
  x = fmaxf(__uint_as_float(s[0]), __uint_as_float(s[1]));
  auto t = __builtin_amdgcn_permlane32_swap(__float_as_uint(x), __float_as_uint(x), false, false);
  return fmaxf(__uint_as_float(t[0]), __uint_as_float(t[1]));
}
static __device__ __forceinline__ f32x2 xrow_scatter(const float (&a)[8]) {
  const float s0 = sw32(a[0], a[4]), s1 = sw32(a[1], a[5]), s2 = sw32(a[2], a[6]), s3 = sw32(a[3], a[7]);
  return f32x2{sw16(s0, s2), sw16(s1, s3)};
}
static __device__ __forceinline__ rsrc_t mkr(const void* p, unsigned bytes) { return __builtin_amdgcn_make_buffer_rsrc((void*)p, 0, bytes, 0x00020000); }
template <int AUX> static __device__ __forceinline__ u32x4 bld(rsrc_t r, unsigned voff, unsigned soff) { return __builtin_amdgcn_raw_buffer_load_b128(r, voff, soff, AUX); }
static __device__ __forceinline__ void fma8(float (&acc)[8], float p, u32x4 v) {
#pragma unroll
  for (int k = 0; k < 4; ++k) { acc[2 * k] = fmaf(p, bflo(v[k]), acc[2 * k]); acc[2 * k + 1] = fmaf(p, bfhi(v[k]), acc[2 * k + 1]); }
}
static __device__ __forceinline__ void zip(u32x4& A, u32x4& B) {
  u32x4 lo, hi;
#pragma unroll
  for (int k = 0; k < 4; ++k) { lo[k] = __builtin_amdgcn_perm(B[k], A[k], 0x05040100u); hi[k] = __builtin_amdgcn_perm(B[k], A[k], 0x07060302u); }
  A = lo; B = hi;
}
static __device__ __forceinline__ void dot8(float (&y)[8], unsigned pr, const u32x4& lo, const u32x4& hi) {
#pragma unroll
  for (int k = 0; k < 4; ++k) { y[2 * k] = dot2(lo[k], pr, y[2 * k]); y[2 * k + 1] = dot2(hi[k], pr, y[2 * k + 1]); }
}
#define WG_BARRIER() do { asm volatile("s_waitcnt lgkmcnt(0)" ::: "memory"); __builtin_amdgcn_s_barrier(); asm volatile("" ::: "memory"); } while (0)
#define PIN8(a) asm volatile("" : "+v"(a[0]), "+v"(a[1]), "+v"(a[2]), "+v"(a[3]), "+v"(a[4]), "+v"(a[5]), "+v"(a[6]), "+v"(a[7]) :: "memory")
#define FENCE() __builtin_amdgcn_sched_barrier(0)

constexpr size_t WS_EPOCH = 0, WS_GRAN = 4096, WS_END = WS_GRAN + (size_t)NB * NH * DM * 8;
constexpr int L_XN = 0;
constexpr int L_QP = L_XN + 4 * 256;
constexpr int L_SC = L_QP + 4 * 128;
constexpr int L_ML = L_SC + 4096;
constexpr int L_HP = L_ML + 8;
constexpr int L_REC = L_HP + 4 * 64;
constexpr int L_KB = L_REC + 4 * 128;
constexpr int L_END = L_KB + 16384;
static_assert(L_KB % 4 == 0 && L_END * 4 <= 160 * 1024, "LDS");

__global__ void __launch_bounds__(512, 2)
fused_decode_kernel(const u16* __restrict__ kc, const u16* __restrict__ x, const u16* __restrict__ wq, unsigned char* __restrict__ ws,
                    const u16* __restrict__ vc, const u16* __restrict__ w1, const u16* __restrict__ w2, const u16* __restrict__ b1,
                    const u16* __restrict__ wo, const u16* __restrict__ b2, u16* __restrict__ out) {
  asm volatile("" :: "s"(kc), "s"(x), "s"(wq), "s"(ws));
  __shared__ __attribute__((aligned(16))) float lds[L_END];
  const int tid = threadIdx.x, lane = tid & 63, w = __builtin_amdgcn_readfirstlane(tid >> 6);
  const int g = lane >> 4, c = lane & 15, rw = w & 3;
  const int bh = blockIdx.x, b = bh >> 5, h = bh & 31;
  unsigned* const epoch_p = reinterpret_cast<unsigned*>(ws + WS_EPOCH) + b * 64;
  u64* const gran = reinterpret_cast<u64*>(ws + WS_GRAN);

  if (__builtin_expect(w < 4, 1)) {
    const char* kb = reinterpret_cast<const char*>(kc) + (size_t)bh * HEAD_BYTES;
    const unsigned voff = lane * 16 + rw * 4096;
    const unsigned tstart = (unsigned)__builtin_amdgcn_s_memrealtime();
    u32x4 R[3][16];
    const float* const kl = lds + L_KB + rw * 4096;
#define LOAD1(S, T, rs, so) R[S][T] = bld<NT>(rs, voff + ((T) & 3) * 1024, (so) + ((T) >> 2) * 16384)
    {
      const rsrc_t rk = mkr(kb, HEAD_BYTES);
#pragma unroll
      for (int s = 0; s < 3; ++s) {
#pragma unroll
        for (int t = 0; t < 16; ++t) { if (s * 16 + t > THR) THROTTLE(); LOAD1(s, t, rk, s * 65536); } }
      FENCE();
      const unsigned kbase = (unsigned)(size_t)(const __attribute__((address_space(3))) float*)kl;
#pragma unroll
      for (int u = 0; u < 4; ++u) {
        unsigned keep;
        asm volatile("s_mov_b32 %0, m0\n\ts_mov_b32 m0, %1\n\t"
                     "s_waitcnt vmcnt(" STR(THR_LDS) ")\n\tbuffer_load_dwordx4 %2, %3, %4 offen nt lds\n\t"
                     "s_waitcnt vmcnt(" STR(THR_LDS) ")\n\tbuffer_load_dwordx4 %2, %3, %4 offen offset:1024 nt lds\n\t"
                     "s_waitcnt vmcnt(" STR(THR_LDS) ")\n\tbuffer_load_dwordx4 %2, %3, %4 offen offset:2048 nt lds\n\t"
                     "s_waitcnt vmcnt(" STR(THR_LDS) ")\n\tbuffer_load_dwordx4 %2, %3, %4 offen offset:3072 nt lds\n\t"
                     "s_mov_b32 m0, %0"
                     : "=&s"(keep) : "s"(kbase + u * 4096), "v"(voff), "s"(rk), "s"(3 * 65536 + u * 16384) : "memory");
      }
    }
    FENCE();
    const char* vb = reinterpret_cast<const char*>(vc) + (size_t)bh * HEAD_BYTES;
    WG_BARRIER();
    u32x4 qpk;
    {
      f32x4 q0 = {0.f, 0.f, 0.f, 0.f}, q1 = {0.f, 0.f, 0.f, 0.f};
#pragma unroll
      for (int i = 0; i < 4; ++i) {
        q0 += *reinterpret_cast<const f32x4*>(lds + L_QP + i * 128 + c * 8);
        q1 += *reinterpret_cast<const f32x4*>(lds + L_QP + i * 128 + c * 8 + 4);
      }
      qpk = u32x4{cvtpk(q0[0], q0[1]), cvtpk(q0[2], q0[3]), cvtpk(q1[0], q1[1]), cvtpk(q1[2], q1[3])};
    }
    constexpr float SCALE = 0.08838834764831845f, LOG2E = 1.4426950408889634f;
    float* const sc = lds + L_SC + rw * 1024;
    float mloc = -INFINITY;
#define DOT4(D, V) { D = 0.f; _Pragma("unroll") for (int k = 0; k < 4; ++k) D = dot2((V)[k], qpk[k], D); }
#define TR_STEP(HALF, CTRL) _Pragma("unroll") for (int k = 0; k < (HALF); ++k) { \
          const float t1_ = xs[k] + dpp<CTRL>(xs[k]), t2_ = xs[k + (HALF)] + dpp<CTRL>(xs[k + (HALF)]); xs[k] = (c & (HALF)) ? t2_ : t1_; }
#define K_FINISH(N) { \
        TR_STEP(8, 0x128) TR_STEP(4, 0x141) TR_STEP(2, 0x4E) TR_STEP(1, 0xB1) \
        const float sv = rbf(rbf(xs[0]) * SCALE) * LOG2E; \
        mloc = fmaxf(mloc, sv); \
        sc[(N) * 64 + lane] = sv; }
#define K_GROUP(N, JN, PS) { \
        const rsrc_t rn = mkr((JN) < 16 ? kb : vb, HEAD_BYTES); \
        const unsigned so = ((JN) & 15) * 65536; \
        float xs[16], ys[16]; \
        const unsigned late = LATE((JN) - 1); \
        _Pragma("unroll") for (int q = 0; q < 16; q += 2) { \
          u32x4 kq[2]; \
          if (PS == 3) { _Pragma("unroll") for (int t = q; t < q + 2; ++t) kq[t - q] = *reinterpret_cast<const u32x4*>(kl + t * 256 + lane * 4); } \
          _Pragma("unroll") for (int t = q; t < q + 2; ++t) { DOT4(xs[t], R[0][t]); asm volatile("" : "+v"(xs[t]) :: "memory"); } \
          FENCE(); \
          _Pragma("unroll") for (int t = q; t < q + 2; ++t) { THROTTLE_LATE(late); LOAD1(0, t, rn, so); } \
          FENCE(); \
          if (PS == 1 || PS == 2) { _Pragma("unroll") for (int t = q; t < q + 2; ++t) { asm volatile("" : "+v"(R[PS % 3][t])); DOT4(ys[t], R[PS % 3][t]); asm volatile("" : "+v"(ys[t])); } \
            FENCE(); } \
          if (PS == 3) { _Pragma("unroll") for (int t = q; t < q + 2; ++t) { DOT4(ys[t], kq[t - q]); asm volatile("" : "+v"(ys[t])); } \
            FENCE(); } \
        } \
        K_FINISH(N); \
        if (PS) { _Pragma("unroll") for (int t = 0; t < 16; ++t) xs[t] = ys[t]; K_FINISH(PS); } \
        FENCE(); }
    K_GROUP(0, 4, 1);
    K_GROUP(4, 5, 2);
    K_GROUP(5, 6, 3);
    for (int j = 6; j < 16; ++j) K_GROUP(j, j + 1, 0);

    const rsrc_t rv = mkr(vb, HEAD_BYTES);
    const float mw = xrow_max(row16_max(mloc));
    {
      const unsigned late = LATE(16);
      float l = 0.f, sv[16];
#pragma unroll
      for (int n = 0; n < 16; ++n) sv[n] = sc[n * 64 + lane];
      FENCE();
#pragma unroll
      for (int n = 0; n < 16; ++n) {
        THROTTLE_LATE(late); LOAD1(1, n, rv, 65536);
        FENCE();
        if (n < 8) {
#pragma unroll
          for (int m = 2 * n; m < 2 * n + 2; ++m) { const float p = __builtin_amdgcn_exp2f(sv[m] - mw); l += p; sc[m * 64 + lane] = p; }
          asm volatile("" : "+v"(l));
        }
        if (n == 8) { l = xrow_sum(row16_sum(l)); if (lane == 0) { lds[L_ML + rw] = mw; lds[L_ML + 4 + rw] = l; } }
        if (n == 10) WG_BARRIER();
        FENCE();
      }
    }
    float acc[8];
#pragma unroll
    for (int k = 0; k < 8; ++k) acc[k] = 0.f;
#define V_GROUP(J, JN, REFILL, RIDE) { \
      const unsigned so = (JN) * 65536; \
      const unsigned late = REFILL ? LATE(15 + (JN)) : 0u; \
      f32x4 pp[4], p1[4]; \
      _Pragma("unroll") for (int q = 0; q < 4; ++q) pp[q] = *reinterpret_cast<const f32x4*>(sc + (J) * 64 + g * 16 + q * 4); \
      if (RIDE) { _Pragma("unroll") for (int q = 0; q < 4; ++q) p1[q] = *reinterpret_cast<const f32x4*>(sc + 64 + g * 16 + q * 4); } \
      _Pragma("unroll") for (int q = 0; q < 16; q += 2) { \
        _Pragma("unroll") for (int t = q; t < q + 2; ++t) fma8(acc, pp[t >> 2][t & 3], R[0][t]); \
        PIN8(acc); \
        FENCE(); \
        if (REFILL) { _Pragma("unroll") for (int t = q; t < q + 2; ++t) { THROTTLE_LATE(late); LOAD1(0, t, rv, so); } } \
        FENCE(); \
        if (RIDE) { _Pragma("unroll") for (int t = q; t < q + 2; ++t) { asm volatile("" : "+v"(R[1][t])); fma8(acc, p1[t >> 2][t & 3], R[1][t]); } PIN8(acc); FENCE(); } \
      } }
    V_GROUP(0, 2, true, true);
    for (int j = 2; j < 15; ++j) V_GROUP(j, j + 1, true, false);
    float fw;
    {
      const f32x4 m4 = *reinterpret_cast<const f32x4*>(lds + L_ML), l4 = *reinterpret_cast<const f32x4*>(lds + L_ML + 4);
      const float M = fmaxf(fmaxf(m4[0], m4[1]), fmaxf(m4[2], m4[3]));
      float L = 0.f;
#pragma unroll
      for (int i = 0; i < 4; ++i) L = fmaf(__builtin_amdgcn_exp2f(m4[i] - M), l4[i], L);
      fw = __builtin_amdgcn_exp2f(mw - M) / L;
      asm volatile("" : "+v"(fw));
      FENCE();
    }
    V_GROUP(15, 0, false, false);
    {
      f32x2 r = xrow_scatter(acc);
      r *= fw;
      *reinterpret_cast<f32x2*>(lds + L_REC + rw * 128 + c * 8 + g * 2) = r;
    }
    WG_BARRIER();
    if (rw == 0) {
      const unsigned tag = reinterpret_cast<const unsigned*>(lds)[L_XN];
      const f32x2 xb = *reinterpret_cast<const f32x2*>(lds + L_XN + 16 + (lane & 7) * 2);
      const rsrc_t rg = mkr(gran + (size_t)bh * 512, 4096);
      __builtin_amdgcn_s_sleep(POLL_FIRST);
      u32x4 t[4];
#pragma unroll 1
      for (unsigned spin = 0; spin < (1u << 16); ++spin) {
        bool ok = true;
#pragma unroll
        for (int k = 0; k < 4; ++k) t[k] = bld<SC1>(rg, lane * 16 + k * 1024, 0);
#pragma unroll
        for (int k = 0; k < 4; ++k) ok &= t[k][1] == tag && t[k][3] == tag;
        if (__all(ok)) break;
        __builtin_amdgcn_s_sleep(POLL_SLEEP);
        asm volatile("" ::: "memory");
      }
      if (h == 0 && lane == 0) __hip_atomic_store(epoch_p, tag, __ATOMIC_RELAXED, __HIP_MEMORY_SCOPE_AGENT);
      FENCE();
      float y0 = (__uint_as_float(t[0][0]) + __uint_as_float(t[1][0])) + (__uint_as_float(t[2][0]) + __uint_as_float(t[3][0]));
      float y1 = (__uint_as_float(t[0][2]) + __uint_as_float(t[1][2])) + (__uint_as_float(t[2][2]) + __uint_as_float(t[3][2]));
      y0 += dpp<0x128>(y0); y1 += dpp<0x128>(y1);
      y0 = xrow_sum(y0); y1 = xrow_sum(y1);
      if (lane < 8) reinterpret_cast<unsigned*>(out)[(b * DM + h * 16) / 2 + lane] = cvtpk(y0 + xb[0], y1 + xb[1]);
    }
  } else {
    __builtin_amdgcn_s_setprio(3);
    const u32x4 xv = *reinterpret_cast<const u32x4*>(x + b * DM + lane * 8);
    u32x4 W[32];
    const rsrc_t rq = mkr(wq + h * DH, DM * WQ_ROW - h * DH * 2);
    const unsigned vwq = (rw * 16 + g * 4) * WQ_ROW + c * 16;
#define LOADWQ(j) { const unsigned so_ = (((j) + b) & 7) * (64 * WQ_ROW); _Pragma("unroll") for (int t_ = 0; t_ < 4; ++t_) W[4 * (j) + t_] = bld<0>(rq, vwq, so_ + t_ * WQ_ROW); }
    LOADWQ(0);
    FENCE();
    unsigned* const xnp = reinterpret_cast<unsigned*>(lds) + L_XN + rw * 256;
    {
      float xf[8];
#pragma unroll
      for (int k = 0; k < 4; ++k) { xf[2 * k] = bflo(xv[k]); xf[2 * k + 1] = bfhi(xv[k]); }
      float s = 0.f;
#pragma unroll
      for (int k = 0; k < 8; ++k) s += xf[k];
      s = xrow_sum(row16_sum(s));
      const float mean = s * (1.f / DM);
      float q = 0.f;
#pragma unroll
      for (int k = 0; k < 8; ++k) { const float d = xf[k] - mean; q = fmaf(d, d, q); }
      q = xrow_sum(row16_sum(q));
      const float mb = rbf(mean), sd = rbf(__builtin_sqrtf(rbf(rbf(q * (1.f / DM)) + 1e-5f)));
#pragma unroll
      for (int k = 0; k < 8; ++k) xf[k] = rbf(xf[k] - mb) / sd;
      *reinterpret_cast<u32x4*>(xnp + lane * 4) = u32x4{cvtpk(xf[0], xf[1]), cvtpk(xf[2], xf[3]), cvtpk(xf[4], xf[5]), cvtpk(xf[6], xf[7])};
    }
    asm volatile("s_waitcnt lgkmcnt(0)" ::: "memory");
    {
      u32x2 xr[8];
#pragma unroll
      for (int j = 0; j < 8; ++j) xr[j] = *reinterpret_cast<const u32x2*>(xnp + ((j + b) & 7) * 32 + rw * 8 + g * 2);
      float a[8];
#pragma unroll
      for (int k = 0; k < 8; ++k) a[k] = 0.f;
      asm volatile("s_waitcnt vmcnt(0)" ::: "memory");
      FENCE();
      LOADWQ(1);
#pragma unroll
      for (int j = 0; j < 8; ++j) {
        FENCE();
        zip(W[4 * j], W[4 * j + 1]); zip(W[4 * j + 2], W[4 * j + 3]);
        asm volatile("" : "+v"(W[4 * j]), "+v"(W[4 * j + 1]), "+v"(W[4 * j + 2]), "+v"(W[4 * j + 3]) :: "memory");
        FENCE();
        if (j >= 1 && j + 1 < 8) LOADWQ(j + 1);
        FENCE();
        dot8(a, xr[j][0], W[4 * j], W[4 * j + 1]); dot8(a, xr[j][1], W[4 * j + 2], W[4 * j + 3]);
      }
      *reinterpret_cast<f32x2*>(lds + L_QP + rw * 128 + c * 8 + g * 2) = xrow_scatter(a);
    }
    WG_BARRIER();
    const unsigned epoch_v = __hip_atomic_load(epoch_p, __ATOMIC_RELAXED, __HIP_MEMORY_SCOPE_AGENT);
    const unsigned tag = __builtin_amdgcn_readfirstlane(epoch_v) * 1664525u + 1013904223u;
    const int sl = (h & 7) * 4 + (h >> 3);
    const int ch0 = sl * 5 + (sl < 12 ? sl : 12), nch = sl < 12 ? 6 : 5;
    {
      const int r = lane >> 3, chl = lane & 7;
      const rsrc_t r1 = mkr(w1 + (size_t)rw * 128 * FF, 128 * FF * 2);
      const unsigned vo = r * 16 * (FF * 2) + (ch0 + (chl < nch ? chl : nch - 1)) * 16;
      u32x4 xr[2];
#pragma unroll
      for (int i = 0; i < 2; ++i) xr[i] = *reinterpret_cast<const u32x4*>(xnp + rw * 64 + r * 8 + i * 4);
      float a[8];
#pragma unroll
      for (int k = 0; k < 8; ++k) a[k] = 0.f;
#pragma unroll
      for (int i = 0; i < 8; ++i) {
        __builtin_amdgcn_s_sleep(PACE);
        W[0] = bld<0>(r1, vo, (2 * i) * (FF * 2)); W[1] = bld<0>(r1, vo, (2 * i + 1) * (FF * 2));
        zip(W[0], W[1]); dot8(a, xr[i >> 2][i & 3], W[0], W[1]);
        PIN8(a);
        FENCE();
      }
      f32x2 r2 = xrow_scatter(a);
      r2[0] += dpp<0x128>(r2[0]); r2[1] += dpp<0x128>(r2[1]);
      if ((lane & 8) == 0) *reinterpret_cast<f32x2*>(lds + L_HP + rw * 64 + chl * 8 + g * 2) = r2;
    }
    asm volatile("" ::: "memory");
    FENCE();
    u32x4 W2[12];
    unsigned b1v;
    {
      const rsrc_t r2 = mkr(w2 + (size_t)ch0 * 8 * DM, nch * 8 * DM * 2);
      const unsigned vo = g * 12 * (DM * 2) + rw * 256 + c * 16;
      b1v = __builtin_amdgcn_raw_buffer_load_b32(mkr(b1 + ch0 * 8, nch * 16), (g * 12 + c * 2) * 2, 0, 0);
#pragma unroll
      for (int i = 0; i < 6; ++i) {
        __builtin_amdgcn_s_sleep(PACE);
        W2[2 * i] = bld<0>(r2, vo + (i & 1) * 2048, (i >> 1) * 4096); W2[2 * i + 1] = bld<0>(r2, vo + (i & 1) * 2048 + 1024, (i >> 1) * 4096);
        zip(W2[2 * i], W2[2 * i + 1]); asm volatile("" : "+v"(W2[2 * i]), "+v"(W2[2 * i + 1]) :: "memory");
        FENCE();
      }
    }
    WG_BARRIER();
    if (rw == 0) {
      const float xb = bflo(x[b * DM + h * 16 + c]) + bflo(b2[h * 16 + c]);
      if (lane < 16) lds[L_XN + 16 + c] = xb;
      if (lane == 0) reinterpret_cast<unsigned*>(lds)[L_XN] = tag;
    }
    float y[8];
    {
      const int f = g * 12 + (c < 6 ? c : 5) * 2;
      f32x2 hs = {0.f, 0.f};
#pragma unroll
      for (int i = 0; i < 4; ++i) hs += *reinterpret_cast<const f32x2*>(lds + L_HP + i * 64 + f);
      const float t0 = rbf(rbf(hs[0]) + bflo(b1v)), t1 = rbf(rbf(hs[1]) + bfhi(b1v));
      const float h0 = t0 > 0.f ? t0 : 0.f, h1 = t1 > 0.f ? t1 : 0.f;
      unsigned hp = cvtpk(h0 * h0, h1 * h1);
      if (f >= nch * 8) hp = 0u;
#pragma unroll
      for (int k = 0; k < 8; ++k) y[k] = 0.f;
      dot8(y, rowbc<0>(hp), W2[0], W2[1]); dot8(y, rowbc<1>(hp), W2[2], W2[3]); dot8(y, rowbc<2>(hp), W2[4], W2[5]);
      dot8(y, rowbc<3>(hp), W2[6], W2[7]); dot8(y, rowbc<4>(hp), W2[8], W2[9]); dot8(y, rowbc<5>(hp), W2[10], W2[11]);
      PIN8(y);
    }
    {
      const rsrc_t ro = mkr(wo + (size_t)h * DH * DM, DH * DM * 2);
      const unsigned vq = g * 32 * (DM * 2) + rw * 256 + c * 16;
#pragma unroll
      for (int i = 0; i < 16; ++i) {
        __builtin_amdgcn_s_sleep(PACE);
        W[2 * i] = bld<0>(ro, vq + (i & 1) * 2048, (i >> 1) * 4096); W[2 * i + 1] = bld<0>(ro, vq + (i & 1) * 2048 + 1024, (i >> 1) * 4096);
        zip(W[2 * i], W[2 * i + 1]); asm volatile("" : "+v"(W[2 * i]), "+v"(W[2 * i + 1]) :: "memory");
        FENCE();
      }
    }
    const rsrc_t rp = mkr(gran + (size_t)(b * 32 + rw * 8) * 512, 8 * 4096);
    const unsigned gpo = (c >> 1) * 4096 + h * 128 + ((c & 1) * 4 + g) * 16;
    WG_BARRIER();
    {
      f32x2 o2 = *reinterpret_cast<const f32x2*>(lds + L_REC + lane * 2);
#pragma unroll
      for (int i = 1; i < 4; ++i) o2 += *reinterpret_cast<const f32x2*>(lds + L_REC + i * 128 + lane * 2);
      const unsigned op = cvtpk(o2[0], o2[1]);
#define PRJ(i) dot8(y, rowbc<i>(op), W[2 * (i)], W[2 * (i) + 1]);
      PRJ(0) PRJ(1) PRJ(2) PRJ(3) PRJ(4) PRJ(5) PRJ(6) PRJ(7) PRJ(8) PRJ(9) PRJ(10) PRJ(11) PRJ(12) PRJ(13) PRJ(14) PRJ(15)
#undef PRJ
      const f32x2 r = xrow_scatter(y);
      __builtin_amdgcn_raw_buffer_store_b128(u32x4{__float_as_uint(r[0]), tag, __float_as_uint(r[1]), tag}, rp, gpo, 0, SC1);
    }
  }
}

extern "C" void kernel_launch(void* const* d_in, const int* in_sizes, int n_in, void* d_out, int out_size, void* d_ws, size_t ws_size, hipStream_t stream) {
  if (n_in != 10 || out_size != NB * DM || ws_size < WS_END || in_sizes[1] != NB * NH * SK * DH || in_sizes[6] != DM * FF) return;
  fused_decode_kernel<<<NB * NH, 512, 0, stream>>>((const u16*)d_in[1], (const u16*)d_in[0], (const u16*)d_in[3], (unsigned char*)d_ws, (const u16*)d_in[2],
                                                   (const u16*)d_in[6], (const u16*)d_in[8], (const u16*)d_in[7], (const u16*)d_in[4], (const u16*)d_in[9], (u16*)d_out);
}
```

```cpp
#include <hip/hip_runtime.h>
#include <hip/hip_bf16.h>
#include <stdint.h>

typedef unsigned u32x4 __attribute__((ext_vector_type(4)));
typedef unsigned u32x2 __attribute__((ext_vector_type(2)));
typedef float f32x4 __attribute__((ext_vector_type(4)));
typedef float f32x2 __attribute__((ext_vector_type(2)));
typedef __bf16 bf16x2 __attribute__((ext_vector_type(2)));
typedef unsigned short u16;
typedef unsigned long long u64;
typedef __amdgpu_buffer_rsrc_t rsrc_t;

constexpr int NB = 8, NH = 32, SK = 4096, DH = 128, DM = 512, FF = 1376;
constexpr unsigned HEAD_BYTES = SK * DH * 2;
constexpr unsigned WQ_ROW = NH * DH * 2;

#define THR 5
#define THR_LO 4
#define THR_HI 7
#define THR_LDS 6
#define SCH_A 380
#define SCH_B 218
#define SCH_LAG 200
#define STR2(x) #x
#define STR(x) STR2(x)
#define THROTTLE() asm volatile("s_waitcnt vmcnt(" STR(THR) ")" ::: "memory")
#define THROTTLE_LATE(late) asm volatile("s_waitcnt vmcnt(" STR(THR_HI) ")\n\ts_cmp_gt_i32 %0, " STR(SCH_LAG) "\n\ts_cbranch_scc1 1f\n\t" \
                                         "s_waitcnt vmcnt(" STR(THR) ")\n\ts_cmp_gt_i32 %0, 0\n\ts_cbranch_scc1 1f\n\t" \
                                         "s_waitcnt vmcnt(" STR(THR_LO) ")\n1:" :: "s"(late) : "memory", "scc")
#define LATE(n) ((unsigned)__builtin_amdgcn_s_memrealtime() - tstart - (unsigned)(SCH_A + SCH_B * (n)))
constexpr int PACE = 16, POLL_FIRST = 32, POLL_SLEEP = 1;
constexpr int SC1 = 16, NT = 2;

static __device__ __forceinline__ float dot2(unsigned a, unsigned b, float c) {
  return __builtin_amdgcn_fdot2_f32_bf16(__builtin_bit_cast(bf16x2, a), __builtin_bit_cast(bf16x2, b), c, false);
}
static __device__ __forceinline__ float bflo(unsigned u) { return __builtin_bit_cast(float, u << 16); }
static __device__ __forceinline__ float bfhi(unsigned u) { return __builtin_bit_cast(float, u & 0xffff0000u); }
static __device__ __forceinline__ unsigned cvtpk(float lo, float hi) {
  bf16x2 r = {(__bf16)lo, (__bf16)hi};
  return __builtin_bit_cast(unsigned, r);
}
static __device__ __forceinline__ float rbf(float x) { return (float)(__bf16)x; }
template <int CTRL> static __device__ __forceinline__ float dpp(float x) {
  return __builtin_bit_cast(float, __builtin_amdgcn_mov_dpp(__builtin_bit_cast(int, x), CTRL, 0xf, 0xf, true));
}
template <int N> static __device__ __forceinline__ unsigned rowbc(unsigned x) {
  return (unsigned)__builtin_amdgcn_mov_dpp((int)x, 0x150 + N, 0xf, 0xf, true);
}
static __device__ __forceinline__ float row16_sum(float x) { x += dpp<0x128>(x); x += dpp<0x124>(x); x += dpp<0x122>(x); x += dpp<0x121>(x); return x; }
static __device__ __forceinline__ float row16_max(float x) { x = fmaxf(x, dpp<0x128>(x)); x = fmaxf(x, dpp<0x124>(x)); x = fmaxf(x, dpp<0x122>(x)); x = fmaxf(x, dpp<0x121>(x)); return x; }
static __device__ __forceinline__ float sw32(float a, float b) {
  auto t = __builtin_amdgcn_permlane32_swap(__float_as_uint(a), __float_as_uint(b), false, false);
  return __uint_as_float(t[0]) + __uint_as_float(t[1]);
}
static __device__ __forceinline__ float sw16(float a, float b) {
  auto t = __builtin_amdgcn_permlane16_swap(__float_as_uint(a), __float_as_uint(b), false, false);
  return __uint_as_float(t[0]) + __uint_as_float(t[1]);
}
static __device__ __forceinline__ float xrow_sum(float x) { x = sw16(x, x); return sw32(x, x); }
static __device__ __forceinline__ float xrow_max(float x) {
  auto s = __builtin_amdgcn_permlane16_swap(__float_as_uint(x), __float_as_uint(x), false, false);
  x = fmaxf(__uint_as_float(s[0]), __uint_as_float(s[1]));
  auto t = __builtin_amdgcn_permlane32_swap(__float_as_uint(x), __float_as_uint(x), false, false);
  return fmaxf(__uint_as_float(t[0]), __uint_as_float(t[1]));
}
static __device__ __forceinline__ f32x2 xrow_scatter(const float (&a)[8]) {
  const float s0 = sw32(a[0], a[4]), s1 = sw32(a[1], a[5]), s2 = sw32(a[2], a[6]), s3 = sw32(a[3], a[7]);
  return f32x2{sw16(s0, s2), sw16(s1, s3)};
}
static __device__ __forceinline__ rsrc_t mkr(const void* p, unsigned bytes) { return __builtin_amdgcn_make_buffer_rsrc((void*)p, 0, bytes, 0x00020000); }
template <int AUX> static __device__ __forceinline__ u32x4 bld(rsrc_t r, unsigned voff, unsigned soff) { return __builtin_amdgcn_raw_buffer_load_b128(r, voff, soff, AUX); }
static __device__ __forceinline__ void fma8(float (&acc)[8], float p, u32x4 v) {
#pragma unroll
  for (int k = 0; k < 4; ++k) { acc[2 * k] = fmaf(p, bflo(v[k]), acc[2 * k]); acc[2 * k + 1] = fmaf(p, bfhi(v[k]), acc[2 * k + 1]); }
}
static __device__ __forceinline__ void zip(u32x4& A, u32x4& B) {
  u32x4 lo, hi;
#pragma unroll
  for (int k = 0; k < 4; ++k) { lo[k] = __builtin_amdgcn_perm(B[k], A[k], 0x05040100u); hi[k] = __builtin_amdgcn_perm(B[k], A[k], 0x07060302u); }
  A = lo; B = hi;
}
static __device__ __forceinline__ void dot8(float (&y)[8], unsigned pr, const u32x4& lo, const u32x4& hi) {
#pragma unroll
  for (int k = 0; k < 4; ++k) { y[2 * k] = dot2(lo[k], pr, y[2 * k]); y[2 * k + 1] = dot2(hi[k], pr, y[2 * k + 1]); }
}
#define WG_BARRIER() do { asm volatile("s_waitcnt lgkmcnt(0)" ::: "memory"); __builtin_amdgcn_s_barrier(); asm volatile("" ::: "memory"); } while (0)
#define PIN8(a) asm volatile("" : "+v"(a[0]), "+v"(a[1]), "+v"(a[2]), "+v"(a[3]), "+v"(a[4]), "+v"(a[5]), "+v"(a[6]), "+v"(a[7]) :: "memory")
#define FENCE() __builtin_amdgcn_sched_barrier(0)

constexpr size_t WS_EPOCH = 0, WS_GRAN = 4096, WS_END = WS_GRAN + (size_t)NB * NH * DM * 8;
constexpr int L_XN = 0;
constexpr int L_QP = L_XN + 4 * 256;
constexpr int L_SC = L_QP + 4 * 128;
constexpr int L_ML = L_SC + 4096;
constexpr int L_HP = L_ML + 8;
constexpr int L_REC = L_HP + 4 * 64;
constexpr int L_KB = L_REC + 4 * 128;
constexpr int L_END = L_KB + 16384;
static_assert(L_KB % 4 == 0 && L_END * 4 <= 160 * 1024, "LDS");

__global__ void __launch_bounds__(512, 2)
fused_decode_kernel(const u16* __restrict__ kc, const u16* __restrict__ x, const u16* __restrict__ wq, unsigned char* __restrict__ ws,
                    const u16* __restrict__ vc, const u16* __restrict__ w1, const u16* __restrict__ w2, const u16* __restrict__ b1,
                    const u16* __restrict__ wo, const u16* __restrict__ b2, u16* __restrict__ out) {
  asm volatile("" :: "s"(kc), "s"(x), "s"(wq), "s"(ws));
  __shared__ __attribute__((aligned(16))) float lds[L_END];
  const int tid = threadIdx.x, lane = tid & 63, w = __builtin_amdgcn_readfirstlane(tid >> 6);
  const int g = lane >> 4, c = lane & 15, rw = w & 3;
  const int bh = blockIdx.x, b = bh >> 5, h = bh & 31;
  unsigned* const epoch_p = reinterpret_cast<unsigned*>(ws + WS_EPOCH) + b * 64;
  u64* const gran = reinterpret_cast<u64*>(ws + WS_GRAN);

  if (__builtin_expect(w < 4, 1)) {
    const char* kb = reinterpret_cast<const char*>(kc) + (size_t)bh * HEAD_BYTES;
    const unsigned voff = lane * 16 + rw * 4096;
    const unsigned tstart = (unsigned)__builtin_amdgcn_s_memrealtime();
    u32x4 R[3][16];
    const float* const kl = lds + L_KB + rw * 4096;
#define LOAD1(S, T, rs, so) R[S][T] = bld<NT>(rs, voff + ((T) & 3) * 1024, (so) + ((T) >> 2) * 16384)
    {
      const rsrc_t rk = mkr(kb, HEAD_BYTES);
#pragma unroll
      for (int s = 0; s < 3; ++s) {
#pragma unroll
        for (int t = 0; t < 16; ++t) { if (s * 16 + t > THR) THROTTLE(); LOAD1(s, t, rk, s * 65536); } }
      FENCE();
      const unsigned kbase = (unsigned)(size_t)(const __attribute__((address_space(3))) float*)kl;
#pragma unroll
      for (int u = 0; u < 4; ++u) {
        unsigned keep;
        asm volatile("s_mov_b32 %0, m0\n\ts_mov_b32 m0, %1\n\t"
                     "s_waitcnt vmcnt(" STR(THR_LDS) ")\n\tbuffer_load_dwordx4 %2, %3, %4 offen nt lds\n\t"
                     "s_waitcnt vmcnt(" STR(THR_LDS) ")\n\tbuffer_load_dwordx4 %2, %3, %4 offen offset:1024 nt lds\n\t"
                     "s_waitcnt vmcnt(" STR(THR_LDS) ")\n\tbuffer_load_dwordx4 %2, %3, %4 offen offset:2048 nt lds\n\t"
                     "s_waitcnt vmcnt(" STR(THR_LDS) ")\n\tbuffer_load_dwordx4 %2, %3, %4 offen offset:3072 nt lds\n\t"
                     "s_mov_b32 m0, %0"
                     : "=&s"(keep) : "s"(kbase + u * 4096), "v"(voff), "s"(rk), "s"(3 * 65536 + u * 16384) : "memory");
      }
    }
    FENCE();
    const char* vb = reinterpret_cast<const char*>(vc) + (size_t)bh * HEAD_BYTES;
    WG_BARRIER();
    u32x4 qpk;
    {
      f32x4 q0 = {0.f, 0.f, 0.f, 0.f}, q1 = {0.f, 0.f, 0.f, 0.f};
#pragma unroll
      for (int i = 0; i < 4; ++i) {
        q0 += *reinterpret_cast<const f32x4*>(lds + L_QP + i * 128 + c * 8);
        q1 += *reinterpret_cast<const f32x4*>(lds + L_QP + i * 128 + c * 8 + 4);
      }
      qpk = u32x4{cvtpk(q0[0], q0[1]), cvtpk(q0[2], q0[3]), cvtpk(q1[0], q1[1]), cvtpk(q1[2], q1[3])};
    }
    constexpr float SCALE = 0.08838834764831845f, LOG2E = 1.4426950408889634f;
    float* const sc = lds + L_SC + rw * 1024;
    float mloc = -INFINITY;
#define DOT4(D, V) { D = 0.f; _Pragma("unroll") for (int k = 0; k < 4; ++k) D = dot2((V)[k], qpk[k], D); }
#define TR_STEP(HALF, CTRL) _Pragma("unroll") for (int k = 0; k < (HALF); ++k) { \
          const float t1_ = xs[k] + dpp<CTRL>(xs[k]), t2_ = xs[k + (HALF)] + dpp<CTRL>(xs[k + (HALF)]); xs[k] = (c & (HALF)) ? t2_ : t1_; }
#define K_FINISH(N) { \
        TR_STEP(8, 0x128) TR_STEP(4, 0x141) TR_STEP(2, 0x4E) TR_STEP(1, 0xB1) \
        const float sv = rbf(rbf(xs[0]) * SCALE) * LOG2E; \
        mloc = fmaxf(mloc, sv); \
        sc[(N) * 64 + lane] = sv; }
#define K_GROUP(N, JN, PS) { \
        const rsrc_t rn = mkr((JN) < 16 ? kb : vb, HEAD_BYTES); \
        const unsigned so = ((JN) & 15) * 65536; \
        float xs[16], ys[16]; \
        const unsigned late = LATE((JN) - 1); \
        _Pragma("unroll") for (int q = 0; q < 16; q += 2) { \
          u32x4 kq[2]; \
          if (PS == 3) { _Pragma("unroll") for (int t = q; t < q + 2; ++t) kq[t - q] = *reinterpret_cast<const u32x4*>(kl + t * 256 + lane * 4); } \
          _Pragma("unroll") for (int t = q; t < q + 2; ++t) { DOT4(xs[t], R[0][t]); asm volatile("" : "+v"(xs[t]) :: "memory"); } \
          FENCE(); \
          _Pragma("unroll") for (int t = q; t < q + 2; ++t) { THROTTLE_LATE(late); LOAD1(0, t, rn, so); } \
          FENCE(); \
          if (PS == 1 || PS == 2) { _Pragma("unroll") for (int t = q; t < q + 2; ++t) { asm volatile("" : "+v"(R[PS % 3][t])); DOT4(ys[t], R[PS % 3][t]); asm volatile("" : "+v"(ys[t])); } \
            FENCE(); } \
          if (PS == 3) { _Pragma("unroll") for (int t = q; t < q + 2; ++t) { DOT4(ys[t], kq[t - q]); asm volatile("" : "+v"(ys[t])); } \
            FENCE(); } \
        } \
        K_FINISH(N); \
        if (PS) { _Pragma("unroll") for (int t = 0; t < 16; ++t) xs[t] = ys[t]; K_FINISH(PS); } \
        FENCE(); }
    K_GROUP(0, 4, 1);
    K_GROUP(4, 5, 2);
    K_GROUP(5, 6, 3);
    for (int j = 6; j < 16; ++j) K_GROUP(j, j + 1, 0);

    const rsrc_t rv = mkr(vb, HEAD_BYTES);
    float fw;
    {
      const float mw = xrow_max(row16_max(mloc));
      const unsigned late = LATE(16);
      float l = 0.f, sv[16];
#pragma unroll
      for (int n = 0; n < 16; ++n) sv[n] = sc[n * 64 + lane];
      FENCE();
#pragma unroll
      for (int n = 0; n < 16; ++n) {
        THROTTLE_LATE(late); LOAD1(1, n, rv, 65536);
        FENCE();
        if (n < 8) {
#pragma unroll
          for (int m = 2 * n; m < 2 * n + 2; ++m) { const float p = __builtin_amdgcn_exp2f(sv[m] - mw); l += p; sc[m * 64 + lane] = p; }
          asm volatile("" : "+v"(l));
        }
        if (n == 8) { l = xrow_sum(row16_sum(l)); if (lane == 0) { lds[L_ML + rw] = mw; lds[L_ML + 4 + rw] = l; } }
        if (n == 10) WG_BARRIER();
        if (n == 12) {
          const f32x4 m4 = *reinterpret_cast<const f32x4*>(lds + L_ML), l4 = *reinterpret_cast<const f32x4*>(lds + L_ML + 4);
          const float M = fmaxf(fmaxf(m4[0], m4[1]), fmaxf(m4[2], m4[3]));
          float L = 0.f;
#pragma unroll
          for (int i = 0; i < 4; ++i) L = fmaf(__builtin_amdgcn_exp2f(m4[i] - M), l4[i], L);
          fw = __builtin_amdgcn_exp2f(mw - M) / L;
          asm volatile("" : "+v"(fw));
        }
        FENCE();
      }
    }
    float acc[8];
#pragma unroll
    for (int k = 0; k < 8; ++k) acc[k] = 0.f;
#define V_GROUP(J, JN, REFILL, RIDE) { \
      const unsigned so = (JN) * 65536; \
      const unsigned late = REFILL ? LATE(15 + (JN)) : 0u; \
      f32x4 pp[4], p1[4]; \
      _Pragma("unroll") for (int q = 0; q < 4; ++q) pp[q] = *reinterpret_cast<const f32x4*>(sc + (J) * 64 + g * 16 + q * 4); \
      if (RIDE) { _Pragma("unroll") for (int q = 0; q < 4; ++q) p1[q] = *reinterpret_cast<const f32x4*>(sc + 64 + g * 16 + q * 4); } \
      _Pragma("unroll") for (int q = 0; q < 16; q += 2) { \
        _Pragma("unroll") for (int t = q; t < q + 2; ++t) fma8(acc, pp[t >> 2][t & 3], R[0][t]); \
        PIN8(acc); \
        FENCE(); \
        if (REFILL) { _Pragma("unroll") for (int t = q; t < q + 2; ++t) { THROTTLE_LATE(late); LOAD1(0, t, rv, so); } } \
        FENCE(); \
        if (RIDE) { _Pragma("unroll") for (int t = q; t < q + 2; ++t) { asm volatile("" : "+v"(R[1][t])); fma8(acc, p1[t >> 2][t & 3], R[1][t]); } PIN8(acc); FENCE(); } \
      } }
    V_GROUP(0, 2, true, true);
    for (int j = 2; j < 15; ++j) V_GROUP(j, j + 1, true, false);
    V_GROUP(15, 0, false, false);
    {
      f32x2 r = xrow_scatter(acc);
      r *= fw;
      *reinterpret_cast<f32x2*>(lds + L_REC + rw * 128 + c * 8 + g * 2) = r;
    }
    WG_BARRIER();
    if (rw == 0) {
      const unsigned tag = reinterpret_cast<const unsigned*>(lds)[L_XN];
      const f32x2 xb = *reinterpret_cast<const f32x2*>(lds + L_XN + 16 + (lane & 7) * 2);
      const rsrc_t rg = mkr(gran + (size_t)bh * 512, 4096);
      __builtin_amdgcn_s_sleep(POLL_FIRST);
      u32x4 t[4];
#pragma unroll 1
      for (unsigned spin = 0; spin < (1u << 16); ++spin) {
        bool ok = true;
#pragma unroll
        for (int k = 0; k < 4; ++k) t[k] = bld<SC1>(rg, lane * 16 + k * 1024, 0);
#pragma unroll
        for (int k = 0; k < 4; ++k) ok &= t[k][1] == tag && t[k][3] == tag;
        if (__all(ok)) break;
        __builtin_amdgcn_s_sleep(POLL_SLEEP);
        asm volatile("" ::: "memory");
      }
      if (h == 0 && lane == 0) __hip_atomic_store(epoch_p, tag, __ATOMIC_RELAXED, __HIP_MEMORY_SCOPE_AGENT);
      FENCE();
      float y0 = (__uint_as_float(t[0][0]) + __uint_as_float(t[1][0])) + (__uint_as_float(t[2][0]) + __uint_as_float(t[3][0]));
      float y1 = (__uint_as_float(t[0][2]) + __uint_as_float(t[1][2])) + (__uint_as_float(t[2][2]) + __uint_as_float(t[3][2]));
      y0 += dpp<0x128>(y0); y1 += dpp<0x128>(y1);
      y0 = xrow_sum(y0); y1 = xrow_sum(y1);
      if (lane < 8) reinterpret_cast<unsigned*>(out)[(b * DM + h * 16) / 2 + lane] = cvtpk(y0 + xb[0], y1 + xb[1]);
    }
  } else {
    __builtin_amdgcn_s_setprio(3);
    const u32x4 xv = *reinterpret_cast<const u32x4*>(x + b * DM + lane * 8);
    u32x4 W[32];
    const rsrc_t rq = mkr(wq + h * DH, DM * WQ_ROW - h * DH * 2);
    const unsigned vwq = (rw * 16 + g * 4) * WQ_ROW + c * 16;
#define LOADWQ(j) { const unsigned so_ = (((j) + b) & 7) * (64 * WQ_ROW); _Pragma("unroll") for (int t_ = 0; t_ < 4; ++t_) W[4 * (j) + t_] = bld<0>(rq, vwq, so_ + t_ * WQ_ROW); }
    LOADWQ(0);
    FENCE();
    unsigned* const xnp = reinterpret_cast<unsigned*>(lds) + L_XN + rw * 256;
    {
      float xf[8];
#pragma unroll
      for (int k = 0; k < 4; ++k) { xf[2 * k] = bflo(xv[k]); xf[2 * k + 1] = bfhi(xv[k]); }
      float s = 0.f;
#pragma unroll
      for (int k = 0; k < 8; ++k) s += xf[k];
      s = xrow_sum(row16_sum(s));
      const float mean = s * (1.f / DM);
      float q = 0.f;
#pragma unroll
      for (int k = 0; k < 8; ++k) { const float d = xf[k] - mean; q = fmaf(d, d, q); }
      q = xrow_sum(row16_sum(q));
      const float mb = rbf(mean), sd = rbf(__builtin_sqrtf(rbf(rbf(q * (1.f / DM)) + 1e-5f)));
#pragma unroll
      for (int k = 0; k < 8; ++k) xf[k] = rbf(xf[k] - mb) / sd;
      *reinterpret_cast<u32x4*>(xnp + lane * 4) = u32x4{cvtpk(xf[0], xf[1]), cvtpk(xf[2], xf[3]), cvtpk(xf[4], xf[5]), cvtpk(xf[6], xf[7])};
    }
    asm volatile("s_waitcnt lgkmcnt(0)" ::: "memory");
    {
      u32x2 xr[8];
#pragma unroll
      for (int j = 0; j < 8; ++j) xr[j] = *reinterpret_cast<const u32x2*>(xnp + ((j + b) & 7) * 32 + rw * 8 + g * 2);
      float a[8];
#pragma unroll
      for (int k = 0; k < 8; ++k) a[k] = 0.f;
      asm volatile("s_waitcnt vmcnt(0)" ::: "memory");
      FENCE();
      LOADWQ(1);
#pragma unroll
      for (int j = 0; j < 8; ++j) {
        FENCE();
        zip(W[4 * j], W[4 * j + 1]); zip(W[4 * j + 2], W[4 * j + 3]);
        asm volatile("" : "+v"(W[4 * j]), "+v"(W[4 * j + 1]), "+v"(W[4 * j + 2]), "+v"(W[4 * j + 3]) :: "memory");
        FENCE();
        if (j >= 1 && j + 1 < 8) LOADWQ(j + 1);
        FENCE();
        dot8(a, xr[j][0], W[4 * j], W[4 * j + 1]); dot8(a, xr[j][1], W[4 * j + 2], W[4 * j + 3]);
      }
      *reinterpret_cast<f32x2*>(lds + L_QP + rw * 128 + c * 8 + g * 2) = xrow_scatter(a);
    }
    WG_BARRIER();
    const unsigned epoch_v = __hip_atomic_load(epoch_p, __ATOMIC_RELAXED, __HIP_MEMORY_SCOPE_AGENT);
    const unsigned tag = __builtin_amdgcn_readfirstlane(epoch_v) * 1664525u + 1013904223u;
    const int sl = (h & 7) * 4 + (h >> 3);
    const int ch0 = sl * 5 + (sl < 12 ? sl : 12), nch = sl < 12 ? 6 : 5;
    {
      const int r = lane >> 3, chl = lane & 7;
      const rsrc_t r1 = mkr(w1 + (size_t)rw * 128 * FF, 128 * FF * 2);
      const unsigned vo = r * 16 * (FF * 2) + (ch0 + (chl < nch ? chl : nch - 1)) * 16;
      u32x4 xr[2];
#pragma unroll
      for (int i = 0; i < 2; ++i) xr[i] = *reinterpret_cast<const u32x4*>(xnp + rw * 64 + r * 8 + i * 4);
      float a[8];
#pragma unroll
      for (int k = 0; k < 8; ++k) a[k] = 0.f;
#pragma unroll
      for (int i = 0; i < 8; ++i) {
        __builtin_amdgcn_s_sleep(PACE);
        W[0] = bld<0>(r1, vo, (2 * i) * (FF * 2)); W[1] = bld<0>(r1, vo, (2 * i + 1) * (FF * 2));
        zip(W[0], W[1]); dot8(a, xr[i >> 2][i & 3], W[0], W[1]);
        PIN8(a);
        FENCE();
      }
      f32x2 r2 = xrow_scatter(a);
      r2[0] += dpp<0x128>(r2[0]); r2[1] += dpp<0x128>(r2[1]);
      if ((lane & 8) == 0) *reinterpret_cast<f32x2*>(lds + L_HP + rw * 64 + chl * 8 + g * 2) = r2;
    }
    asm volatile("" ::: "memory");
    FENCE();
    u32x4 W2[12];
    unsigned b1v;
    {
      const rsrc_t r2 = mkr(w2 + (size_t)ch0 * 8 * DM, nch * 8 * DM * 2);
      const unsigned vo = g * 12 * (DM * 2) + rw * 256 + c * 16;
      b1v = __builtin_amdgcn_raw_buffer_load_b32(mkr(b1 + ch0 * 8, nch * 16), (g * 12 + c * 2) * 2, 0, 0);
#pragma unroll
      for (int i = 0; i < 6; ++i) {
        __builtin_amdgcn_s_sleep(PACE);
        W2[2 * i] = bld<0>(r2, vo + (i & 1) * 2048, (i >> 1) * 4096); W2[2 * i + 1] = bld<0>(r2, vo + (i & 1) * 2048 + 1024, (i >> 1) * 4096);
        zip(W2[2 * i], W2[2 * i + 1]); asm volatile("" : "+v"(W2[2 * i]), "+v"(W2[2 * i + 1]) :: "memory");
        FENCE();
      }
    }
    WG_BARRIER();
    if (rw == 0) {
      const float xb = bflo(x[b * DM + h * 16 + c]) + bflo(b2[h * 16 + c]);
      if (lane < 16) lds[L_XN + 16 + c] = xb;
      if (lane == 0) reinterpret_cast<unsigned*>(lds)[L_XN] = tag;
    }
    float y[8];
    {
      const int f = g * 12 + (c < 6 ? c : 5) * 2;
      f32x2 hs = {0.f, 0.f};
#pragma unroll
      for (int i = 0; i < 4; ++i) hs += *reinterpret_cast<const f32x2*>(lds + L_HP + i * 64 + f);
      const float t0 = rbf(rbf(hs[0]) + bflo(b1v)), t1 = rbf(rbf(hs[1]) + bfhi(b1v));
      const float h0 = t0 > 0.f ? t0 : 0.f, h1 = t1 > 0.f ? t1 : 0.f;
      unsigned hp = cvtpk(h0 * h0, h1 * h1);
      if (f >= nch * 8) hp = 0u;
#pragma unroll
      for (int k = 0; k < 8; ++k) y[k] = 0.f;
      dot8(y, rowbc<0>(hp), W2[0], W2[1]); dot8(y, rowbc<1>(hp), W2[2], W2[3]); dot8(y, rowbc<2>(hp), W2[4], W2[5]);
      dot8(y, rowbc<3>(hp), W2[6], W2[7]); dot8(y, rowbc<4>(hp), W2[8], W2[9]); dot8(y, rowbc<5>(hp), W2[10], W2[11]);
      PIN8(y);
    }
    {
      const rsrc_t ro = mkr(wo + (size_t)h * DH * DM, DH * DM * 2);
      const unsigned vq = g * 32 * (DM * 2) + rw * 256 + c * 16;
#pragma unroll
      for (int i = 0; i < 16; ++i) {
        __builtin_amdgcn_s_sleep(PACE);
        W[2 * i] = bld<0>(ro, vq + (i & 1) * 2048, (i >> 1) * 4096); W[2 * i + 1] = bld<0>(ro, vq + (i & 1) * 2048 + 1024, (i >> 1) * 4096);
        zip(W[2 * i], W[2 * i + 1]); asm volatile("" : "+v"(W[2 * i]), "+v"(W[2 * i + 1]) :: "memory");
        FENCE();
      }
    }
    const rsrc_t rp = mkr(gran + (size_t)(b * 32 + rw * 8) * 512, 8 * 4096);
    const unsigned gpo = (c >> 1) * 4096 + h * 128 + ((c & 1) * 4 + g) * 16;
    WG_BARRIER();
    {
      f32x2 o2 = *reinterpret_cast<const f32x2*>(lds + L_REC + lane * 2);
#pragma unroll
      for (int i = 1; i < 4; ++i) o2 += *reinterpret_cast<const f32x2*>(lds + L_REC + i * 128 + lane * 2);
      const unsigned op = cvtpk(o2[0], o2[1]);
#define PRJ(i) dot8(y, rowbc<i>(op), W[2 * (i)], W[2 * (i) + 1]);
      PRJ(0) PRJ(1) PRJ(2) PRJ(3) PRJ(4) PRJ(5) PRJ(6) PRJ(7) PRJ(8) PRJ(9) PRJ(10) PRJ(11) PRJ(12) PRJ(13) PRJ(14) PRJ(15)
#undef PRJ
      const f32x2 r = xrow_scatter(y);
      __builtin_amdgcn_raw_buffer_store_b128(u32x4{__float_as_uint(r[0]), tag, __float_as_uint(r[1]), tag}, rp, gpo, 0, SC1);
    }
  }
}

extern "C" void kernel_launch(void* const* d_in, const int* in_sizes, int n_in, void* d_out, int out_size, void* d_ws, size_t ws_size, hipStream_t stream) {
  if (n_in != 10 || out_size != NB * DM || ws_size < WS_END || in_sizes[1] != NB * NH * SK * DH || in_sizes[6] != DM * FF) return;
  fused_decode_kernel<<<NB * NH, 512, 0, stream>>>((const u16*)d_in[1], (const u16*)d_in[0], (const u16*)d_in[3], (unsigned char*)d_ws, (const u16*)d_in[2],
                                                   (const u16*)d_in[6], (const u16*)d_in[8], (const u16*)d_in[7], (const u16*)d_in[4], (const u16*)d_in[9], (u16*)d_out);
}
```
